```python
import torch

def _softmax(x, axis=-1):
    return torch.softmax(x, dim=axis)
def _gelu(x):
    return torch.nn.functional.gelu(x, approximate="tanh")
def _silu(x):
    return torch.nn.functional.silu(x)
def _sigmoid(x):
    return torch.sigmoid(x)
def _layernorm(x, axis=-1, eps=1e-5):
    m = x.mean(dim=axis, keepdim=True)
    v = x.var(dim=axis, keepdim=True, unbiased=False)
    return (x - m) / torch.sqrt(v + eps)
def _scatter_add(tgt, idx, src, axis=0):
    return tgt.clone().index_add_(axis, idx, src)
def _segment_sum(src, seg, n):
    out = torch.zeros((n,) + src.shape[1:], dtype=src.dtype)
    return out.index_add_(0, seg, src)
def _pad(x, pw):
    flat = tuple(v for pair in reversed(pw) for v in pair)
    return torch.nn.functional.pad(x, flat)

kernel_name = 'parallel_sum__sliding_local_dilated__glu_gated_reglu__prompt'

def setup_inputs():

    _w = (lambda a: (lambda t: t.to(torch.bfloat16) if t.is_floating_point() else t)(torch.as_tensor(a)))
    def _A_setup():
        B, S, D = 1, 2048, 640
        return {
            "x": _w((lambda *s: torch.randn(*s, dtype=torch.bfloat16) * 0.05)(B, S, D)),
            "wqkv": _w((lambda *s: torch.randn(*s, dtype=torch.bfloat16) * 0.05)(D, 3 * D)),
            "wo": _w((lambda *s: torch.randn(*s, dtype=torch.bfloat16) * 0.05)(D, D)),
        }
    _A_in = _A_setup()

    def _B_setup():
        B, S, D, F = 1, 2048, 640, 1728
        return {
            "x": _w((lambda *s: torch.randn(*s, dtype=torch.bfloat16) * 0.05)(B, S, D)),
            "W_gate": _w((lambda *s: torch.randn(*s, dtype=torch.bfloat16) * 0.8)(D, F) * (2.0 / D) ** 0.5),
            "W_up": _w((lambda *s: torch.randn(*s, dtype=torch.bfloat16) * 0.8)(D, F) * (2.0 / D) ** 0.5),
            "W_down": _w((lambda *s: torch.randn(*s, dtype=torch.bfloat16) * 0.8)(F, D) * (1.0 / F) ** 0.5),
        }
    _B_in = _B_setup()

    return {
        "A_x": _A_in["x"],
        "A_wqkv": _A_in["wqkv"],
        "A_wo": _A_in["wo"],
        "B_x": _B_in["x"],
        "B_W_gate": _B_in["W_gate"],
        "B_W_up": _B_in["W_up"],
        "B_W_down": _B_in["W_down"],
    }

def reference(A_x, A_wqkv, A_wo, B_x, B_W_gate, B_W_up, B_W_down):
    def _A_ref(x, wqkv, wo):
        B, S, D = x.shape
        H = 32; Dh = D // H
        W, dil = 8, 2
        qkv = (lambda x, *d: x.permute(*d))((x @ wqkv).reshape(B, S, 3, H, Dh), 2, 0, 3, 1, 4)
        q, k, v = qkv[0], qkv[1], qkv[2]
        scale = Dh ** -0.5
        pos = torch.arange(S)
        off = torch.arange(W) * dil
        idx = pos[:, None] - off[None, :]
        mask = idx >= 0
        idx_c = torch.where(mask, idx, 0).reshape(-1)
        kg = k[:, :, idx_c, :].reshape(B, H, S, W, Dh)
        vg = v[:, :, idx_c, :].reshape(B, H, S, W, Dh)
        logits = torch.einsum("bhsd,bhswd->bhsw", q, kg) * scale
        logits = torch.where(mask[None, None, :, :], logits, -1e9)
        attn = _softmax(logits, axis=-1)
        o = torch.einsum("bhsw,bhswd->bhsd", attn, vg)
        return (lambda x, *d: x.permute(*d))(o, 0, 2, 1, 3).reshape(B, S, D) @ wo

    def _B_ref(x, W_gate, W_up, W_down):
        gate = torch.einsum("bsd,df->bsf", x, W_gate)
        up = torch.einsum("bsd,df->bsf", x, W_up)
        gate = gate * (lambda x, dt: x.to(dt))(gate > 0, gate.dtype)
        h = gate * up
        return torch.einsum("bsf,fd->bsd", h, W_down)

    x = A_x
    xn = _layernorm(x, axis=-1)
    return x + _A_ref(xn, A_wqkv, A_wo) + _B_ref(xn, B_W_gate, B_W_up, B_W_down)
```

```cpp
#include <hip/hip_runtime.h>
#include <hip/hip_bf16.h>
#include <stdint.h>

typedef unsigned short u16;
typedef short s16x8 __attribute__((ext_vector_type(8)));
typedef short s16x4 __attribute__((ext_vector_type(4)));
typedef float f32x4 __attribute__((ext_vector_type(4)));

#define S_ 2048
#define D_ 640
#define F_ 1728
#define NH 32
#define DH 20

__device__ __forceinline__ float bf2f(u16 v){ return __uint_as_float(((unsigned)v)<<16); }
__device__ __forceinline__ u16 f2bf(float f){ __hip_bfloat16 b = __float2bfloat16(f); return *(u16*)&b; }
__device__ __forceinline__ float rbf(float f){ return bf2f(f2bf(f)); }

__global__ void ln_kernel(const u16* x, u16* xn){
  int row = blockIdx.x; int lane = threadIdx.x;
  const u16* xr = x + (size_t)row*D_;
  float v[10]; float s=0;
  for(int i=0;i<10;i++){ v[i]=bf2f(xr[lane+64*i]); s+=v[i]; }
  for(int o=32;o>0;o>>=1) s+=__shfl_xor(s,o);
  float mean = s/D_;
  float q=0; for(int i=0;i<10;i++){ float d=v[i]-mean; q+=d*d; }
  for(int o=32;o>0;o>>=1) q+=__shfl_xor(q,o);
  float var = q/D_;
  float mb = rbf(mean), vb = rbf(var);
  float den = rbf(sqrtf(rbf(vb+1e-5f)));
  for(int i=0;i<10;i++){ xn[(size_t)row*D_+lane+64*i] = f2bf(rbf(v[i]-mb)/den); }
}

__global__ void gemm_naive(const u16* A, int lda, const u16* B, int ldb, u16* C, int ldc, int K){
  int n = blockIdx.x*64 + threadIdx.x; int m = blockIdx.y;
  float acc=0;
  for(int k=0;k<K;k++) acc += bf2f(A[(size_t)m*lda+k])*bf2f(B[(size_t)k*ldb+n]);
  C[(size_t)m*ldc+n]=f2bf(acc);
}
__global__ void hmul(const u16* g, const u16* u, u16* h){
  int i = blockIdx.x*256+threadIdx.x;
  float gv=bf2f(g[i]); gv = gv>0? gv:0.f;
  h[i]=f2bf(gv*bf2f(u[i]));
}
__global__ void attn_kernel(const u16* qkv, u16* o){
  int s = blockIdx.x; int h = threadIdx.x;
  const u16* q = qkv + (size_t)s*1920 + h*DH;
  float lg[8]; float scale = rsqrtf(20.f);
  scale = 0.22360679774997896f;
  for(int w=0;w<8;w++){
    int p = s-2*w;
    if(p>=0){
      const u16* k = qkv + (size_t)p*1920 + 640 + h*DH;
      float a=0; for(int d=0;d<DH;d++) a+=bf2f(q[d])*bf2f(k[d]);
      lg[w]=rbf(rbf(a)*rbf(scale));
    } else lg[w]=rbf(-1e9f);
  }
  float mx=lg[0]; for(int w=1;w<8;w++) mx=fmaxf(mx,lg[w]);
  float e[8]; float sum=0; for(int w=0;w<8;w++){ e[w]=__expf(lg[w]-mx); sum+=e[w]; }
  for(int w=0;w<8;w++) e[w]=rbf(e[w]/sum);
  for(int d=0;d<DH;d++){
    float a=0;
    for(int w=0;w<8;w++){ int p=s-2*w; if(p<0)p=0; a+=e[w]*bf2f(qkv[(size_t)p*1920+1280+h*DH+d]); }
    o[(size_t)s*D_+h*DH+d]=f2bf(a);
  }
}
__global__ void final_add(const u16* x, const u16* a, const u16* b, u16* out){
  int i = blockIdx.x*256+threadIdx.x;
  out[i]=f2bf(rbf(bf2f(x[i])+bf2f(a[i]))+bf2f(b[i]));
}

extern "C" void kernel_launch(void* const* d_in, const int* in_sizes, int n_in,
                              void* d_out, int out_size, void* d_ws, size_t ws_size,
                              hipStream_t stream) {
  const u16* x=(const u16*)d_in[0]; const u16* wqkv=(const u16*)d_in[1]; const u16* wo=(const u16*)d_in[2];
  const u16* wg=(const u16*)d_in[4]; const u16* wu=(const u16*)d_in[5]; const u16* wd=(const u16*)d_in[6];
  u16* ws=(u16*)d_ws;
  u16* xn=ws; u16* qkv=xn+S_*D_; u16* g=qkv+S_*1920; u16* u=g+S_*F_; u16* h=u+S_*F_; u16* o=h+S_*F_; u16* a=o+S_*D_; u16* b=a+S_*D_;
  ln_kernel<<<S_,64,0,stream>>>(x,xn);
  gemm_naive<<<dim3(30,S_),64,0,stream>>>(xn,D_,wqkv,1920,qkv,1920,D_);
  gemm_naive<<<dim3(27,S_),64,0,stream>>>(xn,D_,wg,F_,g,F_,D_);
  gemm_naive<<<dim3(27,S_),64,0,stream>>>(xn,D_,wu,F_,u,F_,D_);
  hmul<<<S_*F_/256,256,0,stream>>>(g,u,h);
  attn_kernel<<<S_,32,0,stream>>>(qkv,o);
  gemm_naive<<<dim3(10,S_),64,0,stream>>>(o,D_,wo,D_,a,D_,D_);
  gemm_naive<<<dim3(10,S_),64,0,stream>>>(h,F_,wd,D_,b,D_,F_);
  final_add<<<S_*D_/256,256,0,stream>>>(x,a,b,(u16*)d_out);
}
```

```cpp
#include <hip/hip_runtime.h>
#include <hip/hip_bf16.h>
#include <stdint.h>

typedef unsigned short u16;
typedef short s16x8 __attribute__((ext_vector_type(8)));
typedef short s16x4 __attribute__((ext_vector_type(4)));
typedef float f32x4 __attribute__((ext_vector_type(4)));
typedef unsigned u32x4 __attribute__((ext_vector_type(4)));
typedef unsigned u32x2 __attribute__((ext_vector_type(2)));
#define LDS_PTR(T) __attribute__((address_space(3))) T*

#define S_ 2048
#define D_ 640
#define F_ 1728
#define DH 20

__device__ __forceinline__ float bf2f(u16 v){ return __uint_as_float(((unsigned)v)<<16); }
__device__ __forceinline__ u16 f2bf(float f){ __hip_bfloat16 b = __float2bfloat16(f); return *(u16*)&b; }
__device__ __forceinline__ float rbf(float f){ return bf2f(f2bf(f)); }
__device__ __forceinline__ unsigned pk2(float a, float b){ return (unsigned)f2bf(a) | ((unsigned)f2bf(b)<<16); }
__device__ __forceinline__ float lo16(unsigned v){ return __uint_as_float(v<<16); }
__device__ __forceinline__ float hi16(unsigned v){ return __uint_as_float(v&0xffff0000u); }

__device__ __forceinline__ s16x4 tr_read(const char* p){
  return __builtin_amdgcn_ds_read_tr16_b64_v4i16((LDS_PTR(s16x4))p);
}

#define K1_ASZ 16384
#define K1_BSZ 24576
#define K1_STG (K1_ASZ+K1_BSZ)
__global__ __launch_bounds__(256,2) void k1(const u16* __restrict__ x, const u16* __restrict__ wqkv,
    const u16* __restrict__ wg, const u16* __restrict__ wu, u16* __restrict__ qkv, u16* __restrict__ h){
  __shared__ __attribute__((aligned(16))) char smem[2*K1_STG];
  const int tid=threadIdx.x, lane=tid&63, wid=tid>>6;
  const int bid=blockIdx.x;
  const int nt = bid % 28, mt = bid / 28;
  const int m0 = mt*128;
  const bool isq = nt<10;

  {
    float* st = (float*)smem;
    int sub = lane>>4, l16 = lane&15;
    for(int it=0; it<8; ++it){
      int row = wid*32 + it*4 + sub;
      const u16* xr = x + (size_t)(m0+row)*D_;
      float v[40]; float s=0;
      #pragma unroll
      for(int i=0;i<5;i++){
        u32x4 d = *(const u32x4*)(xr + (l16 + 16*i)*8);
        #pragma unroll
        for(int j=0;j<4;j++){ v[i*8+2*j]=lo16(d[j]); v[i*8+2*j+1]=hi16(d[j]); }
      }
      #pragma unroll
      for(int i=0;i<40;i++) s+=v[i];
      #pragma unroll
      for(int o=8;o>0;o>>=1) s+=__shfl_xor(s,o);
      float mean=s*(1.0f/D_);
      float q=0;
      #pragma unroll
      for(int i=0;i<40;i++){ float d=v[i]-mean; q+=d*d; }
      #pragma unroll
      for(int o=8;o>0;o>>=1) q+=__shfl_xor(q,o);
      float mb=rbf(mean), vb=rbf(q*(1.0f/D_));
      float den=rbf(sqrtf(rbf(vb+1e-5f)));
      if(l16==0){ st[row*2]=mb; st[row*2+1]=1.0f/den; }
    }
  }
  __syncthreads();
  float mb[4], ri[4];
  const int ca = tid&7;
  #pragma unroll
  for(int i=0;i<4;i++){ int row=(tid>>3)+32*i; mb[i]=((float*)smem)[row*2]; ri[i]=((float*)smem)[row*2+1]; }
  __syncthreads();

  const u16* bsrc[6]; int ldb = isq?1920:F_;
  #pragma unroll
  for(int i=0;i<6;i++){
    int L = ((wid*6+i)*64+lane)*16;
    int row = L/384, pc=(L%384)/16;
    int sw = ((row>>1)&1) | (((row>>3)&1)<<1);
    int c = (pc>>1)^sw;
    int col = c*16 + (pc&1)*8;
    const u16* p;
    if(isq) p = wqkv + nt*192 + col;
    else { int j=nt-10; int wc=col/96, cc=col%96; p = (cc<48)? (wg + j*96+wc*48+cc) : (wu + j*96+wc*48+cc-48); }
    bsrc[i] = p + (size_t)row*ldb;
  }
  const u16* asrc = x + (size_t)(m0 + (tid>>3))*D_ + ca*8;

  u32x4 areg[4];
  auto loadA=[&](int kt){
    #pragma unroll
    for(int i=0;i<4;i++) areg[i] = *(const u32x4*)(asrc + (size_t)i*32*D_ + kt*64);
  };
  auto writeA=[&](char* buf){
    #pragma unroll
    for(int i=0;i<4;i++){
      int row=(tid>>3)+32*i;
      u32x4 o;
      #pragma unroll
      for(int j=0;j<4;j++) o[j]=pk2(rbf(lo16(areg[i][j])-mb[i])*ri[i], rbf(hi16(areg[i][j])-mb[i])*ri[i]);
      *(u32x4*)(buf + row*128 + ((ca^((row>>1)&7))*16)) = o;
    }
  };
  auto issueB=[&](int kt, char* buf){
    #pragma unroll
    for(int i=0;i<6;i++)
      __builtin_amdgcn_global_load_lds((const void*)(bsrc[i] + (size_t)kt*64*ldb), (LDS_PTR(void))(buf + K1_ASZ + (wid*6+i)*1024 + lane*16), 16, 0, 0);
  };

  f32x4 acc[4][6];
  #pragma unroll
  for(int a=0;a<4;a++)
    #pragma unroll
    for(int b=0;b<6;b++) acc[a][b]=(f32x4){0,0,0,0};

  const int wr=wid>>1, wc=wid&1;
  const int g=lane>>4, l15=lane&15, q=(lane&15)>>2, p=lane&3;

  issueB(0, smem); loadA(0); writeA(smem);
  asm volatile("s_waitcnt vmcnt(0)":::"memory");
  __syncthreads();
  for(int kt=0; kt<10; ++kt){
    char* cur = smem + (kt&1)*K1_STG;
    char* nxt = smem + ((kt&1)^1)*K1_STG;
    if(kt<9){ issueB(kt+1, nxt); loadA(kt+1); }
    #pragma unroll
    for(int ks=0; ks<2; ++ks){
      s16x8 af[4];
      #pragma unroll
      for(int a=0;a<4;a++){
        int row = wr*64 + a*16 + l15;
        int ch = ks*4+g;
        af[a] = *(const s16x8*)(cur + row*128 + ((ch^((row>>1)&7))*16));
      }
      #pragma unroll
      for(int b=0;b<6;b++){
        int c = wc*6+b;
        s16x4 t0,t1;
        { int row=ks*32+8*g+q; int sw=((row>>1)&1)|(((row>>3)&1)<<1);
          t0=tr_read(cur+K1_ASZ+row*384+((c^sw)*32)+p*8); }
        { int row=ks*32+8*g+4+q; int sw=((row>>1)&1)|(((row>>3)&1)<<1);
          t1=tr_read(cur+K1_ASZ+row*384+((c^sw)*32)+p*8); }
        s16x8 bf = __builtin_shufflevector(t0,t1,0,1,2,3,4,5,6,7);
        #pragma unroll
        for(int a=0;a<4;a++)
          acc[a][b]=__builtin_amdgcn_mfma_f32_16x16x32_bf16(bf, af[a], acc[a][b],0,0,0);
      }
    }
    if(kt<9){ writeA(nxt); }
    asm volatile("s_waitcnt vmcnt(0)":::"memory");
    __syncthreads();
  }
  if(isq){
    #pragma unroll
    for(int a=0;a<4;a++){
      int m = m0 + wr*64 + a*16 + l15;
      #pragma unroll
      for(int b=0;b<6;b++){
        int n = nt*192 + wc*96 + b*16 + g*4;
        u32x2 o; o[0]=pk2(acc[a][b][0],acc[a][b][1]); o[1]=pk2(acc[a][b][2],acc[a][b][3]);
        *(u32x2*)(qkv + (size_t)m*1920 + n) = o;
      }
    }
  } else {
    int j=nt-10;
    #pragma unroll
    for(int a=0;a<4;a++){
      int m = m0 + wr*64 + a*16 + l15;
      #pragma unroll
      for(int b=0;b<3;b++){
        int n = j*96 + wc*48 + b*16 + g*4;
        float r[4];
        #pragma unroll
        for(int e=0;e<4;e++){ float gv=rbf(acc[a][b][e]); gv=gv>0.f?gv:0.f; r[e]=gv*rbf(acc[a][b+3][e]); }
        u32x2 o; o[0]=pk2(r[0],r[1]); o[1]=pk2(r[2],r[3]);
        *(u32x2*)(h + (size_t)m*F_ + n) = o;
      }
    }
  }
}

__global__ __launch_bounds__(256) void attn_kernel(const u16* __restrict__ qkv, u16* __restrict__ o){
  int s = blockIdx.x*8 + (threadIdx.x>>5); int h = threadIdx.x&31;
  const u16* qp = qkv + (size_t)s*1920 + h*DH;
  float qv[DH];
  #pragma unroll
  for(int d=0;d<DH;d+=2){ unsigned t=*(const unsigned*)(qp+d); qv[d]=lo16(t); qv[d+1]=hi16(t); }
  float lg[8]; const float scale = 0.2236328125f;
  #pragma unroll
  for(int w=0;w<8;w++){
    int p = s-2*w;
    if(p>=0){
      const u16* k = qkv + (size_t)p*1920 + 640 + h*DH;
      float a=0;
      #pragma unroll
      for(int d=0;d<DH;d+=2){ unsigned t=*(const unsigned*)(k+d); a+=qv[d]*lo16(t); a+=qv[d+1]*hi16(t); }
      lg[w]=rbf(rbf(a)*scale);
    } else lg[w]=-1e9f;
  }
  float mx=lg[0];
  #pragma unroll
  for(int w=1;w<8;w++) mx=fmaxf(mx,lg[w]);
  float e[8]; float sum=0;
  #pragma unroll
  for(int w=0;w<8;w++){ e[w]=__expf(lg[w]-mx); sum+=e[w]; }
  #pragma unroll
  for(int w=0;w<8;w++) e[w]=rbf(e[w]/sum);
  float a[DH];
  #pragma unroll
  for(int d=0;d<DH;d++) a[d]=0;
  #pragma unroll
  for(int w=0;w<8;w++){ int p=s-2*w; if(p<0)p=0; const u16* v=qkv+(size_t)p*1920+1280+h*DH;
    #pragma unroll
    for(int d=0;d<DH;d+=2){ unsigned t=*(const unsigned*)(v+d); a[d]+=e[w]*lo16(t); a[d+1]+=e[w]*hi16(t); } }
  #pragma unroll
  for(int d=0;d<DH;d+=2) *(unsigned*)(o+(size_t)s*D_+h*DH+d)=pk2(a[d],a[d+1]);
}

#define K3_ASZ 8192
#define K3_BSZ 10240
#define K3_STG (K3_ASZ+K3_BSZ)
#define K3_NS 3
__global__ __launch_bounds__(256,2) void k3(const u16* __restrict__ x, const u16* __restrict__ o, const u16* __restrict__ h,
    const u16* __restrict__ wo, const u16* __restrict__ wd, u16* __restrict__ out){
  __shared__ __attribute__((aligned(16))) char smem[K3_NS*K3_STG];
  const int tid=threadIdx.x, lane=tid&63, wid=tid>>6;
  const int nt = blockIdx.x & 7, mt = blockIdx.x >> 3;
  const int m0=mt*64, n0=nt*80;
  int arow[2], ach[2];
  #pragma unroll
  for(int i=0;i<2;i++){ int r=(wid*2+i)*8 + (lane>>3); arow[i]=r; ach[i]=((lane&7)^((r>>1)&7))*8; }
  int brow[3], bcol[3];
  #pragma unroll
  for(int i=0;i<3;i++){ int idx = wid + 4*i; int L=(idx*64+lane)*16; brow[i]=L/160; bcol[i]=(L%160)/2; }
  auto issue=[&](int kt, char* buf){
    const u16* A; int lda, k0; const u16* B;
    if(kt<10){ A=o; lda=D_; k0=kt*64; B=wo+(size_t)k0*D_; } else { A=h; lda=F_; k0=(kt-10)*64; B=wd+(size_t)k0*D_; }
    #pragma unroll
    for(int i=0;i<2;i++)
      __builtin_amdgcn_global_load_lds((const void*)(A+(size_t)(m0+arow[i])*lda+k0+ach[i]), (LDS_PTR(void))(buf+(wid*2+i)*1024+lane*16),16,0,0);
    #pragma unroll
    for(int i=0;i<3;i++){ if(wid+4*i<10)
      __builtin_amdgcn_global_load_lds((const void*)(B+(size_t)brow[i]*D_+n0+bcol[i]), (LDS_PTR(void))(buf+K3_ASZ+(wid+4*i)*1024+lane*16),16,0,0); }
  };
  f32x4 acc[2][5];
  #pragma unroll
  for(int a=0;a<2;a++)
    #pragma unroll
    for(int b=0;b<5;b++) acc[a][b]=(f32x4){0,0,0,0};
  const int wr=wid&1, ks=wid>>1;
  const int g=lane>>4, l15=lane&15, q=(lane&15)>>2, p=lane&3;
  issue(0,smem); issue(1,smem+K3_STG);
  for(int kt=0;kt<37;++kt){
    char* cur=smem+(kt%K3_NS)*K3_STG;
    if(kt<36){ if(wid<2) asm volatile("s_waitcnt vmcnt(5)":::"memory"); else asm volatile("s_waitcnt vmcnt(4)":::"memory"); }
    else asm volatile("s_waitcnt vmcnt(0)":::"memory");
    __builtin_amdgcn_s_barrier();
    if(kt+2<37) issue(kt+2, smem+((kt+2)%K3_NS)*K3_STG);
    s16x8 af[2];
    #pragma unroll
    for(int a=0;a<2;a++){ int row=wr*32+a*16+l15; int ch=ks*4+g; af[a]=*(const s16x8*)(cur+row*128+((ch^((row>>1)&7))*16)); }
    #pragma unroll
    for(int b=0;b<5;b++){
      int row=ks*32+8*g+q;
      s16x4 t0=tr_read(cur+K3_ASZ+row*160+b*32+p*8);
      s16x4 t1=tr_read(cur+K3_ASZ+(row+4)*160+b*32+p*8);
      s16x8 bf=__builtin_shufflevector(t0,t1,0,1,2,3,4,5,6,7);
      #pragma unroll
      for(int a=0;a<2;a++) acc[a][b]=__builtin_amdgcn_mfma_f32_16x16x32_bf16(bf,af[a],acc[a][b],0,0,0);
    }
    asm volatile("s_waitcnt lgkmcnt(0)":::"memory");
  }
  __syncthreads();
  float* red=(float*)smem;
  if(ks==1){
    #pragma unroll
    for(int a=0;a<2;a++)
      #pragma unroll
      for(int b=0;b<5;b++) *(f32x4*)(red + ((wr*10+a*5+b)*64+lane)*4)=acc[a][b];
  }
  __syncthreads();
  if(ks==0){
    #pragma unroll
    for(int a=0;a<2;a++){
      int m=m0+wr*32+a*16+l15;
      #pragma unroll
      for(int b=0;b<5;b++){
        f32x4 r=*(const f32x4*)(red + ((wr*10+a*5+b)*64+lane)*4);
        int n=n0+b*16+g*4;
        u32x2 xv=*(const u32x2*)(x+(size_t)m*D_+n);
        u32x2 ov; ov[0]=pk2(lo16(xv[0])+acc[a][b][0]+r[0], hi16(xv[0])+acc[a][b][1]+r[1]);
        ov[1]=pk2(lo16(xv[1])+acc[a][b][2]+r[2], hi16(xv[1])+acc[a][b][3]+r[3]);
        *(u32x2*)(out+(size_t)m*D_+n)=ov;
      }
    }
  }
}

extern "C" void kernel_launch(void* const* d_in, const int* in_sizes, int n_in,
                              void* d_out, int out_size, void* d_ws, size_t ws_size,
                              hipStream_t stream) {
  const u16* x=(const u16*)d_in[0]; const u16* wqkv=(const u16*)d_in[1]; const u16* wo=(const u16*)d_in[2];
  const u16* wg=(const u16*)d_in[4]; const u16* wu=(const u16*)d_in[5]; const u16* wd=(const u16*)d_in[6];
  u16* ws=(u16*)d_ws;
  u16* qkv=ws; u16* h=qkv+S_*1920; u16* o=h+S_*F_;
  k1<<<16*28,256,0,stream>>>(x,wqkv,wg,wu,qkv,h);
  attn_kernel<<<S_/8,256,0,stream>>>(qkv,o);
  k3<<<256,256,0,stream>>>(x,o,h,wo,wd,(u16*)d_out);
}
```

```cpp
#include <hip/hip_runtime.h>
#include <hip/hip_bf16.h>
#include <stdint.h>

typedef unsigned short u16;
typedef short s16x8 __attribute__((ext_vector_type(8)));
typedef short s16x4 __attribute__((ext_vector_type(4)));
typedef float f32x4 __attribute__((ext_vector_type(4)));
typedef unsigned u32x4 __attribute__((ext_vector_type(4)));
typedef unsigned u32x2 __attribute__((ext_vector_type(2)));
#define LDS_PTR(T) __attribute__((address_space(3))) T*

#define S_ 2048
#define D_ 640
#define F_ 1728
#define DH 20

__device__ __forceinline__ float bf2f(u16 v){ return __uint_as_float(((unsigned)v)<<16); }
__device__ __forceinline__ u16 f2bf(float f){ __hip_bfloat16 b = __float2bfloat16(f); return *(u16*)&b; }
__device__ __forceinline__ float rbf(float f){ return bf2f(f2bf(f)); }
__device__ __forceinline__ unsigned pk2(float a, float b){ return (unsigned)f2bf(a) | ((unsigned)f2bf(b)<<16); }
__device__ __forceinline__ float lo16(unsigned v){ return __uint_as_float(v<<16); }
__device__ __forceinline__ float hi16(unsigned v){ return __uint_as_float(v&0xffff0000u); }

__device__ __forceinline__ s16x4 tr_read(const char* p){
  return __builtin_amdgcn_ds_read_tr16_b64_v4i16((LDS_PTR(s16x4))p);
}


__device__ __forceinline__ void glds16(const void* gsrc, unsigned lds_dst){
  unsigned keep;
  asm volatile("s_mov_b32 %0, m0\n\ts_mov_b32 m0, %2\n\ts_nop 0\n\tglobal_load_lds_dwordx4 %1, off\n\ts_mov_b32 m0, %0" : "=&s"(keep) : "v"(gsrc), "s"(lds_dst) : "memory");
}
#define LADDR(p) ((unsigned)__builtin_amdgcn_readfirstlane((int)(unsigned)(size_t)(p)))
__global__ __launch_bounds__(256) void ln_kernel(const u16* __restrict__ x, u16* __restrict__ xn){
  int lane=threadIdx.x&63; int row=blockIdx.x*16+(threadIdx.x>>6)*4+(lane>>4); int l16=lane&15;
  const u16* xr=x+(size_t)row*D_;
  float v[40]; float s=0;
  #pragma unroll
  for(int i=0;i<5;i++){ u32x4 d=*(const u32x4*)(xr+(l16+16*i)*8);
    #pragma unroll
    for(int j=0;j<4;j++){ v[i*8+2*j]=lo16(d[j]); v[i*8+2*j+1]=hi16(d[j]); } }
  #pragma unroll
  for(int i=0;i<40;i++) s+=v[i];
  #pragma unroll
  for(int o=8;o>0;o>>=1) s+=__shfl_xor(s,o);
  float mean=s*(1.0f/D_); float q=0;
  #pragma unroll
  for(int i=0;i<40;i++){ float d=v[i]-mean; q+=d*d; }
  #pragma unroll
  for(int o=8;o>0;o>>=1) q+=__shfl_xor(q,o);
  float mb=rbf(mean), vb=rbf(q*(1.0f/D_));
  float ri=1.0f/rbf(sqrtf(rbf(vb+1e-5f)));
  #pragma unroll
  for(int i=0;i<5;i++){ u32x4 o;
    #pragma unroll
    for(int j=0;j<4;j++) o[j]=pk2(rbf(v[i*8+2*j]-mb)*ri, rbf(v[i*8+2*j+1]-mb)*ri);
    *(u32x4*)(xn+(size_t)row*D_+(l16+16*i)*8)=o; }
}
#define K1_ASZ 16384
#define K1_BSZ 24576
#define K1_STG (K1_ASZ+K1_BSZ)
#define K1_NS 3
__global__ __launch_bounds__(256,1) void k1(const u16* __restrict__ xn, const u16* __restrict__ wqkv,
    const u16* __restrict__ wg, const u16* __restrict__ wu, u16* __restrict__ qkv, u16* __restrict__ h){
  __shared__ __attribute__((aligned(16))) char smem[K1_NS*K1_STG];
  const int tid=threadIdx.x, lane=tid&63, wid=tid>>6;
  const int bid=blockIdx.x;
  const int xcd=bid&7, idx=bid>>3;
  const int mt=(xcd&1)*8+(idx&7), nt=(xcd>>1)*7+(idx>>3);
  const int m0 = mt*128;
  const bool isq = nt<10;
  const u16* bsrc[6]; int ldb = isq?1920:F_;
  #pragma unroll
  for(int i=0;i<6;i++){
    int L = ((wid*6+i)*64+lane)*16;
    int row = L/384, pc=(L%384)/16;
    int sw = ((row>>1)&1) | (((row>>3)&1)<<1);
    int c = (pc>>1)^sw;
    int col = c*16 + (pc&1)*8;
    const u16* p;
    if(isq) p = wqkv + nt*192 + col;
    else { int j=nt-10; int wc=col/96, cc=col%96; p = (cc<48)? (wg + j*96+wc*48+cc) : (wu + j*96+wc*48+cc-48); }
    bsrc[i] = p + (size_t)row*ldb;
  }
  const u16* asrc[4];
  #pragma unroll
  for(int i=0;i<4;i++){ int r=(wid*4+i)*8+(lane>>3); asrc[i]=xn+(size_t)(m0+r)*D_+(((lane&7)^((r>>1)&7))*8); }
  auto issue=[&](int kt, char* buf){
    #pragma unroll
    for(int i=0;i<4;i++)
      glds16((const void*)(asrc[i]+kt*64), LADDR(buf+(wid*4+i)*1024));
    #pragma unroll
    for(int i=0;i<6;i++)
      glds16((const void*)(bsrc[i] + (size_t)kt*64*ldb), LADDR(buf + K1_ASZ + (wid*6+i)*1024));
  };
  f32x4 acc[4][6];
  #pragma unroll
  for(int a=0;a<4;a++)
    #pragma unroll
    for(int b=0;b<6;b++) acc[a][b]=(f32x4){0,0,0,0};
  const int wr=wid>>1, wc=wid&1;
  const int g=lane>>4, l15=lane&15, q=(lane&15)>>2, p=lane&3;
  issue(0,smem); issue(1,smem+K1_STG);
  for(int kt=0; kt<10; ++kt){
    char* cur = smem + (kt%K1_NS)*K1_STG;
    if(kt<9) asm volatile("s_waitcnt vmcnt(10)":::"memory"); else asm volatile("s_waitcnt vmcnt(0)":::"memory");
    __builtin_amdgcn_s_barrier();
    if(kt+2<10) issue(kt+2, smem+((kt+2)%K1_NS)*K1_STG);
    #pragma unroll
    for(int ks=0; ks<2; ++ks){
      s16x8 af[4];
      #pragma unroll
      for(int a=0;a<4;a++){
        int row = wr*64 + a*16 + l15;
        int ch = ks*4+g;
        af[a] = *(const s16x8*)(cur + row*128 + ((ch^((row>>1)&7))*16));
      }
      #pragma unroll
      for(int b=0;b<6;b++){
        int c = wc*6+b;
        s16x4 t0,t1;
        { int row=ks*32+8*g+q; int sw=((row>>1)&1)|(((row>>3)&1)<<1);
          t0=tr_read(cur+K1_ASZ+row*384+((c^sw)*32)+p*8); }
        { int row=ks*32+8*g+4+q; int sw=((row>>1)&1)|(((row>>3)&1)<<1);
          t1=tr_read(cur+K1_ASZ+row*384+((c^sw)*32)+p*8); }
        s16x8 bf = __builtin_shufflevector(t0,t1,0,1,2,3,4,5,6,7);
        #pragma unroll
        for(int a=0;a<4;a++)
          acc[a][b]=__builtin_amdgcn_mfma_f32_16x16x32_bf16(bf, af[a], acc[a][b],0,0,0);
      }
    }
    asm volatile("s_waitcnt lgkmcnt(0)":::"memory");
  }
  if(isq){
    #pragma unroll
    for(int a=0;a<4;a++){
      int m = m0 + wr*64 + a*16 + l15;
      #pragma unroll
      for(int b=0;b<6;b++){
        int n = nt*192 + wc*96 + b*16 + g*4;
        u32x2 o; o[0]=pk2(acc[a][b][0],acc[a][b][1]); o[1]=pk2(acc[a][b][2],acc[a][b][3]);
        *(u32x2*)(qkv + (size_t)m*1920 + n) = o;
      }
    }
  } else {
    int j=nt-10;
    #pragma unroll
    for(int a=0;a<4;a++){
      int m = m0 + wr*64 + a*16 + l15;
      #pragma unroll
      for(int b=0;b<3;b++){
        int n = j*96 + wc*48 + b*16 + g*4;
        float r[4];
        #pragma unroll
        for(int e=0;e<4;e++){ float gv=rbf(acc[a][b][e]); gv=gv>0.f?gv:0.f; r[e]=gv*rbf(acc[a][b+3][e]); }
        u32x2 o; o[0]=pk2(r[0],r[1]); o[1]=pk2(r[2],r[3]);
        *(u32x2*)(h + (size_t)m*F_ + n) = o;
      }
    }
  }
}

__global__ __launch_bounds__(256) void attn_kernel(const u16* __restrict__ qkv, u16* __restrict__ o){
  int s = blockIdx.x*8 + (threadIdx.x>>5); int h = threadIdx.x&31;
  const u16* qp = qkv + (size_t)s*1920 + h*DH;
  float qv[DH];
  #pragma unroll
  for(int d=0;d<DH;d+=2){ unsigned t=*(const unsigned*)(qp+d); qv[d]=lo16(t); qv[d+1]=hi16(t); }
  float lg[8]; const float scale = 0.2236328125f;
  #pragma unroll
  for(int w=0;w<8;w++){
    int p = s-2*w;
    if(p>=0){
      const u16* k = qkv + (size_t)p*1920 + 640 + h*DH;
      float a=0;
      #pragma unroll
      for(int d=0;d<DH;d+=2){ unsigned t=*(const unsigned*)(k+d); a+=qv[d]*lo16(t); a+=qv[d+1]*hi16(t); }
      lg[w]=rbf(rbf(a)*scale);
    } else lg[w]=-1e9f;
  }
  float mx=lg[0];
  #pragma unroll
  for(int w=1;w<8;w++) mx=fmaxf(mx,lg[w]);
  float e[8]; float sum=0;
  #pragma unroll
  for(int w=0;w<8;w++){ e[w]=__expf(lg[w]-mx); sum+=e[w]; }
  #pragma unroll
  for(int w=0;w<8;w++) e[w]=rbf(e[w]/sum);
  float a[DH];
  #pragma unroll
  for(int d=0;d<DH;d++) a[d]=0;
  #pragma unroll
  for(int w=0;w<8;w++){ int p=s-2*w; if(p<0)p=0; const u16* v=qkv+(size_t)p*1920+1280+h*DH;
    #pragma unroll
    for(int d=0;d<DH;d+=2){ unsigned t=*(const unsigned*)(v+d); a[d]+=e[w]*lo16(t); a[d+1]+=e[w]*hi16(t); } }
  #pragma unroll
  for(int d=0;d<DH;d+=2) *(unsigned*)(o+(size_t)s*D_+h*DH+d)=pk2(a[d],a[d+1]);
}

#define K3_ASZ 8192
#define K3_BSZ 10240
#define K3_STG (K3_ASZ+K3_BSZ)
#define K3_NS 8
__global__ __launch_bounds__(256,1) void k3(const u16* __restrict__ x, const u16* __restrict__ o, const u16* __restrict__ h,
    const u16* __restrict__ wo, const u16* __restrict__ wd, u16* __restrict__ out){
  __shared__ __attribute__((aligned(16))) char smem[K3_NS*K3_STG];
  const int tid=threadIdx.x, lane=tid&63, wid=tid>>6;
  const int nt = blockIdx.x >> 5, mt = (blockIdx.x&7)*4 + ((blockIdx.x>>3)&3);
  const int m0=mt*64, n0=nt*80;
  int arow[2], ach[2];
  #pragma unroll
  for(int i=0;i<2;i++){ int r=(wid*2+i)*8 + (lane>>3); arow[i]=r; ach[i]=((lane&7)^((r>>1)&7))*8; }
  int brow[3], bcol[3];
  #pragma unroll
  for(int i=0;i<3;i++){ int idx = wid + 4*i; int L=(idx*64+lane)*16; brow[i]=L/160; bcol[i]=(L%160)/2; }
  auto issue=[&](int kt, char* buf){
    const u16* A; int lda, k0; const u16* B;
    if(kt<10){ A=o; lda=D_; k0=kt*64; B=wo+(size_t)k0*D_; } else { A=h; lda=F_; k0=(kt-10)*64; B=wd+(size_t)k0*D_; }
    #pragma unroll
    for(int i=0;i<2;i++)
      glds16((const void*)(A+(size_t)(m0+arow[i])*lda+k0+ach[i]), LADDR(buf+(wid*2+i)*1024));
    #pragma unroll
    for(int i=0;i<3;i++){ if(wid+4*i<10)
      glds16((const void*)(B+(size_t)brow[i]*D_+n0+bcol[i]), LADDR(buf+K3_ASZ+(wid+4*i)*1024)); }
  };
  f32x4 acc[2][5];
  #pragma unroll
  for(int a=0;a<2;a++)
    #pragma unroll
    for(int b=0;b<5;b++) acc[a][b]=(f32x4){0,0,0,0};
  const int wr=wid&1, ks=wid>>1;
  const int g=lane>>4, l15=lane&15, q=(lane&15)>>2, p=lane&3;
  for(int i=0;i<7;i++) issue(i,smem+i*K3_STG);
  for(int kt=0;kt<37;++kt){
    char* cur=smem+(kt%K3_NS)*K3_STG;
    if(kt<31){ if(wid<2) asm volatile("s_waitcnt vmcnt(30)":::"memory"); else asm volatile("s_waitcnt vmcnt(24)":::"memory"); }
    else asm volatile("s_waitcnt vmcnt(0)":::"memory");
    __builtin_amdgcn_s_barrier();
    if(kt+7<37) issue(kt+7, smem+((kt+7)%K3_NS)*K3_STG);
    s16x8 af[2];
    #pragma unroll
    for(int a=0;a<2;a++){ int row=wr*32+a*16+l15; int ch=ks*4+g; af[a]=*(const s16x8*)(cur+row*128+((ch^((row>>1)&7))*16)); }
    #pragma unroll
    for(int b=0;b<5;b++){
      int row=ks*32+8*g+q;
      s16x4 t0=tr_read(cur+K3_ASZ+row*160+b*32+p*8);
      s16x4 t1=tr_read(cur+K3_ASZ+(row+4)*160+b*32+p*8);
      s16x8 bf=__builtin_shufflevector(t0,t1,0,1,2,3,4,5,6,7);
      #pragma unroll
      for(int a=0;a<2;a++) acc[a][b]=__builtin_amdgcn_mfma_f32_16x16x32_bf16(bf,af[a],acc[a][b],0,0,0);
    }
    asm volatile("s_waitcnt lgkmcnt(0)":::"memory");
  }
  __syncthreads();
  float* red=(float*)smem;
  if(ks==1){
    #pragma unroll
    for(int a=0;a<2;a++)
      #pragma unroll
      for(int b=0;b<5;b++) *(f32x4*)(red + ((wr*10+a*5+b)*64+lane)*4)=acc[a][b];
  }
  __syncthreads();
  if(ks==0){
    #pragma unroll
    for(int a=0;a<2;a++){
      int m=m0+wr*32+a*16+l15;
      #pragma unroll
      for(int b=0;b<5;b++){
        f32x4 r=*(const f32x4*)(red + ((wr*10+a*5+b)*64+lane)*4);
        int n=n0+b*16+g*4;
        u32x2 xv=*(const u32x2*)(x+(size_t)m*D_+n);
        u32x2 ov; ov[0]=pk2(lo16(xv[0])+acc[a][b][0]+r[0], hi16(xv[0])+acc[a][b][1]+r[1]);
        ov[1]=pk2(lo16(xv[1])+acc[a][b][2]+r[2], hi16(xv[1])+acc[a][b][3]+r[3]);
        *(u32x2*)(out+(size_t)m*D_+n)=ov;
      }
    }
  }
}

extern "C" void kernel_launch(void* const* d_in, const int* in_sizes, int n_in,
                              void* d_out, int out_size, void* d_ws, size_t ws_size,
                              hipStream_t stream) {
  const u16* x=(const u16*)d_in[0]; const u16* wqkv=(const u16*)d_in[1]; const u16* wo=(const u16*)d_in[2];
  const u16* wg=(const u16*)d_in[4]; const u16* wu=(const u16*)d_in[5]; const u16* wd=(const u16*)d_in[6];
  u16* ws=(u16*)d_ws;
  u16* qkv=ws; u16* h=qkv+S_*1920; u16* o=h+S_*F_; u16* xn=o+S_*D_;
  ln_kernel<<<128,256,0,stream>>>(x,xn);
  k1<<<16*28,256,0,stream>>>(xn,wqkv,wg,wu,qkv,h);
  attn_kernel<<<S_/8,256,0,stream>>>(qkv,o);
  k3<<<256,256,0,stream>>>(x,o,h,wo,wd,(u16*)d_out);
}
```

```cpp
#include <hip/hip_runtime.h>
#include <hip/hip_bf16.h>
#include <stdint.h>

typedef unsigned short u16;
typedef short s16x8 __attribute__((ext_vector_type(8)));
typedef short s16x4 __attribute__((ext_vector_type(4)));
typedef float f32x4 __attribute__((ext_vector_type(4)));
typedef unsigned u32x4 __attribute__((ext_vector_type(4)));
typedef unsigned u32x2 __attribute__((ext_vector_type(2)));
#define LDS_PTR(T) __attribute__((address_space(3))) T*

#define S_ 2048
#define D_ 640
#define F_ 1728
#define DH 20

__device__ __forceinline__ float bf2f(u16 v){ return __uint_as_float(((unsigned)v)<<16); }
__device__ __forceinline__ u16 f2bf(float f){ __hip_bfloat16 b = __float2bfloat16(f); return *(u16*)&b; }
__device__ __forceinline__ float rbf(float f){ return bf2f(f2bf(f)); }
__device__ __forceinline__ unsigned pk2(float a, float b){ return (unsigned)f2bf(a) | ((unsigned)f2bf(b)<<16); }
__device__ __forceinline__ float lo16(unsigned v){ return __uint_as_float(v<<16); }
__device__ __forceinline__ float hi16(unsigned v){ return __uint_as_float(v&0xffff0000u); }

__device__ __forceinline__ s16x4 tr_read(const char* p){
  return __builtin_amdgcn_ds_read_tr16_b64_v4i16((LDS_PTR(s16x4))p);
}

__device__ __forceinline__ void glds16(const void* gsrc, unsigned lds_dst){
  unsigned keep;
  asm volatile("s_mov_b32 %0, m0\n\ts_mov_b32 m0, %2\n\ts_nop 0\n\tglobal_load_lds_dwordx4 %1, off\n\ts_mov_b32 m0, %0" : "=&s"(keep) : "v"(gsrc), "s"(lds_dst) : "memory");
}
#define LADDR(p) ((unsigned)__builtin_amdgcn_readfirstlane((int)(unsigned)(size_t)(p)))
__global__ __launch_bounds__(256) void ln_kernel(const u16* __restrict__ x, u16* __restrict__ xn){
  int lane=threadIdx.x&63; int row=blockIdx.x*16+(threadIdx.x>>6)*4+(lane>>4); int l16=lane&15;
  const u16* xr=x+(size_t)row*D_;
  float v[40]; float s=0;
  #pragma unroll
  for(int i=0;i<5;i++){ u32x4 d=*(const u32x4*)(xr+(l16+16*i)*8);
    #pragma unroll
    for(int j=0;j<4;j++){ v[i*8+2*j]=lo16(d[j]); v[i*8+2*j+1]=hi16(d[j]); } }
  #pragma unroll
  for(int i=0;i<40;i++) s+=v[i];
  #pragma unroll
  for(int o=8;o>0;o>>=1) s+=__shfl_xor(s,o);
  float mean=s*(1.0f/D_); float q=0;
  #pragma unroll
  for(int i=0;i<40;i++){ float d=v[i]-mean; q+=d*d; }
  #pragma unroll
  for(int o=8;o>0;o>>=1) q+=__shfl_xor(q,o);
  float mb=rbf(mean), vb=rbf(q*(1.0f/D_));
  float ri=1.0f/rbf(sqrtf(rbf(vb+1e-5f)));
  #pragma unroll
  for(int i=0;i<5;i++){ u32x4 o;
    #pragma unroll
    for(int j=0;j<4;j++) o[j]=pk2(rbf(v[i*8+2*j]-mb)*ri, rbf(v[i*8+2*j+1]-mb)*ri);
    *(u32x4*)(xn+(size_t)row*D_+(l16+16*i)*8)=o; }
}
#define K1_ASZ 32768
#define K1_BSZ 24576
#define K1_STG (K1_ASZ+K1_BSZ)
__global__ __launch_bounds__(512,2) void k1(const u16* __restrict__ x, const u16* __restrict__ wqkv,
    const u16* __restrict__ wg, const u16* __restrict__ wu, u16* __restrict__ qkv, u16* __restrict__ h){
  __shared__ __attribute__((aligned(16))) char smem[2*K1_STG];
  const int tid=threadIdx.x, lane=tid&63, wid=tid>>6;
  const int bid=blockIdx.x;
  const int xcd=bid&7, idx=bid>>3;
  const int mt=(xcd&1)*4+(idx&3), nt=(xcd>>1)*7+(idx>>2);
  const int m0 = mt*256;
  const bool isq = nt<10;
  const u16* bsrc[3]; int ldb = isq?1920:F_;
  #pragma unroll
  for(int i=0;i<3;i++){
    int L = ((wid*3+i)*64+lane)*16;
    int row = L/384, pc=(L%384)/16;
    int sw = ((row>>1)&1) | (((row>>3)&1)<<1);
    int c = (pc>>1)^sw;
    int col = c*16 + (pc&1)*8;
    const u16* p;
    if(isq) p = wqkv + nt*192 + col;
    else { int j=nt-10; int wc=col/96, cc=col%96; p = (cc<48)? (wg + j*96+wc*48+cc) : (wu + j*96+wc*48+cc-48); }
    bsrc[i] = p + (size_t)row*ldb;
  }
  auto issueB=[&](int kt, char* buf){
    #pragma unroll
    for(int i=0;i<3;i++)
      glds16((const void*)(bsrc[i] + (size_t)kt*64*ldb), LADDR(buf + K1_ASZ + (wid*3+i)*1024));
  };
  issueB(0,smem);
  {
    float* st=(float*)(smem+K1_STG);
    const int sub=lane>>4, l16=lane&15;
    #pragma unroll
    for(int bt=0;bt<2;bt++){
      u32x4 d[4][5];
      #pragma unroll
      for(int it=0;it<4;it++){ const u16* xr=x+(size_t)(m0+wid*32+bt*16+it*4+sub)*D_;
        #pragma unroll
        for(int i=0;i<5;i++) d[it][i]=*(const u32x4*)(xr+(l16+16*i)*8); }
      #pragma unroll
      for(int it=0;it<4;it++){
        float sm=0;
        #pragma unroll
        for(int i=0;i<5;i++)
          #pragma unroll
          for(int j=0;j<4;j++) sm+=lo16(d[it][i][j])+hi16(d[it][i][j]);
        #pragma unroll
        for(int o=8;o>0;o>>=1) sm+=__shfl_xor(sm,o);
        float mean=sm*(1.0f/D_), qq=0;
        #pragma unroll
        for(int i=0;i<5;i++)
          #pragma unroll
          for(int j=0;j<4;j++){ float a=lo16(d[it][i][j])-mean, b=hi16(d[it][i][j])-mean; qq+=a*a+b*b; }
        #pragma unroll
        for(int o=8;o>0;o>>=1) qq+=__shfl_xor(qq,o);
        float mbv=rbf(mean), vb=rbf(qq*(1.0f/D_));
        float den=rbf(sqrtf(rbf(vb+1e-5f)));
        int row=wid*32+bt*16+it*4+sub;
        if(l16==0){ st[row*2]=mbv; st[row*2+1]=1.0f/den; }
      }
    }
  }
  __syncthreads();
  float mb[4], ri[4];
  const int ca=tid&7;
  #pragma unroll
  for(int i=0;i<4;i++){ int row=(tid>>3)+64*i; mb[i]=((float*)(smem+K1_STG))[row*2]; ri[i]=((float*)(smem+K1_STG))[row*2+1]; }
  __syncthreads();
  const u16* asrc = x + (size_t)(m0+(tid>>3))*D_ + ca*8;
  u32x4 areg[4];
  auto loadA=[&](int kt){
    #pragma unroll
    for(int i=0;i<4;i++) areg[i]=*(const u32x4*)(asrc+(size_t)i*64*D_+kt*64);
  };
  auto writeA=[&](char* buf){
    #pragma unroll
    for(int i=0;i<4;i++){
      int row=(tid>>3)+64*i; u32x4 o;
      #pragma unroll
      for(int j=0;j<4;j++) o[j]=pk2(rbf(lo16(areg[i][j])-mb[i])*ri[i], rbf(hi16(areg[i][j])-mb[i])*ri[i]);
      *(u32x4*)(buf+row*128+((ca^((row>>1)&7))*16))=o;
    }
  };
  f32x4 acc[4][6];
  #pragma unroll
  for(int a=0;a<4;a++)
    #pragma unroll
    for(int b=0;b<6;b++) acc[a][b]=(f32x4){0,0,0,0};
  const int wr=wid>>1, wc=wid&1;
  const int g=lane>>4, l15=lane&15, q=(lane&15)>>2, p=lane&3;
  loadA(0); writeA(smem);
  #pragma unroll
  for(int kt=0; kt<10; ++kt){
    char* cur = smem + (kt&1)*K1_STG;
    asm volatile("s_waitcnt vmcnt(0) lgkmcnt(0)":::"memory");
    __builtin_amdgcn_s_barrier();
    if(kt+1<10){ issueB(kt+1, smem+((kt+1)&1)*K1_STG); loadA(kt+1); }
    #pragma unroll
    for(int ks=0; ks<2; ++ks){
      s16x8 af[4];
      #pragma unroll
      for(int a=0;a<4;a++){
        int row = wr*64 + a*16 + l15;
        int ch = ks*4+g;
        af[a] = *(const s16x8*)(cur + row*128 + ((ch^((row>>1)&7))*16));
      }
      #pragma unroll
      for(int b=0;b<6;b++){
        int c = wc*6+b;
        s16x4 t0,t1;
        { int row=ks*32+8*g+q; int sw=((row>>1)&1)|(((row>>3)&1)<<1);
          t0=tr_read(cur+K1_ASZ+row*384+((c^sw)*32)+p*8); }
        { int row=ks*32+8*g+4+q; int sw=((row>>1)&1)|(((row>>3)&1)<<1);
          t1=tr_read(cur+K1_ASZ+row*384+((c^sw)*32)+p*8); }
        s16x8 bf = __builtin_shufflevector(t0,t1,0,1,2,3,4,5,6,7);
        #pragma unroll
        for(int a=0;a<4;a++)
          acc[a][b]=__builtin_amdgcn_mfma_f32_16x16x32_bf16(bf, af[a], acc[a][b],0,0,0);
      }
    }
    if(kt+1<10) writeA(smem+((kt+1)&1)*K1_STG);
  }
  asm volatile("s_waitcnt lgkmcnt(0)":::"memory");
  __builtin_amdgcn_s_barrier();
  char* eb = smem + wid*(64*208);
  if(isq){
    #pragma unroll
    for(int a=0;a<4;a++)
      #pragma unroll
      for(int b=0;b<6;b++){
        u32x2 o; o[0]=pk2(acc[a][b][0],acc[a][b][1]); o[1]=pk2(acc[a][b][2],acc[a][b][3]);
        *(u32x2*)(eb + (a*16+l15)*208 + b*32 + g*8) = o;
      }
    __builtin_amdgcn_s_waitcnt(0); __builtin_amdgcn_wave_barrier();
    #pragma unroll
    for(int i=0;i<12;i++){ int id=i*64+lane; int r=id/12, c=id%12;
      u32x4 v=*(const u32x4*)(eb + r*208 + c*16);
      *(u32x4*)(qkv + (size_t)(m0+wr*64+r)*1920 + nt*192 + wc*96 + c*8)=v; }
  } else {
    int j=nt-10;
    #pragma unroll
    for(int a=0;a<4;a++)
      #pragma unroll
      for(int b=0;b<3;b++){
        float r[4];
        #pragma unroll
        for(int e=0;e<4;e++){ float gv=rbf(acc[a][b][e]); gv=gv>0.f?gv:0.f; r[e]=gv*rbf(acc[a][b+3][e]); }
        u32x2 o; o[0]=pk2(r[0],r[1]); o[1]=pk2(r[2],r[3]);
        *(u32x2*)(eb + (a*16+l15)*208 + b*32 + g*8) = o;
      }
    __builtin_amdgcn_s_waitcnt(0); __builtin_amdgcn_wave_barrier();
    #pragma unroll
    for(int i=0;i<6;i++){ int id=i*64+lane; int r=id/6, c=id%6;
      u32x4 v=*(const u32x4*)(eb + r*208 + c*16);
      *(u32x4*)(h + (size_t)(m0+wr*64+r)*F_ + j*96 + wc*48 + c*8)=v; }
  }
}

__global__ __launch_bounds__(256) void attn_kernel(const u16* __restrict__ qkv, u16* __restrict__ o){
  int s = blockIdx.x*8 + (threadIdx.x>>5); int h = threadIdx.x&31;
  const u16* qp = qkv + (size_t)s*1920 + h*DH;
  float qv[DH];
  #pragma unroll
  for(int d=0;d<DH;d+=2){ unsigned t=*(const unsigned*)(qp+d); qv[d]=lo16(t); qv[d+1]=hi16(t); }
  float lg[8]; const float scale = 0.2236328125f;
  #pragma unroll
  for(int w=0;w<8;w++){
    int p = s-2*w;
    if(p>=0){
      const u16* k = qkv + (size_t)p*1920 + 640 + h*DH;
      float a=0;
      #pragma unroll
      for(int d=0;d<DH;d+=2){ unsigned t=*(const unsigned*)(k+d); a+=qv[d]*lo16(t); a+=qv[d+1]*hi16(t); }
      lg[w]=rbf(rbf(a)*scale);
    } else lg[w]=-1e9f;
  }
  float mx=lg[0];
  #pragma unroll
  for(int w=1;w<8;w++) mx=fmaxf(mx,lg[w]);
  float e[8]; float sum=0;
  #pragma unroll
  for(int w=0;w<8;w++){ e[w]=__expf(lg[w]-mx); sum+=e[w]; }
  #pragma unroll
  for(int w=0;w<8;w++) e[w]=rbf(e[w]/sum);
  float a[DH];
  #pragma unroll
  for(int d=0;d<DH;d++) a[d]=0;
  #pragma unroll
  for(int w=0;w<8;w++){ int p=s-2*w; if(p<0)p=0; const u16* v=qkv+(size_t)p*1920+1280+h*DH;
    #pragma unroll
    for(int d=0;d<DH;d+=2){ unsigned t=*(const unsigned*)(v+d); a[d]+=e[w]*lo16(t); a[d+1]+=e[w]*hi16(t); } }
  #pragma unroll
  for(int d=0;d<DH;d+=2) *(unsigned*)(o+(size_t)s*D_+h*DH+d)=pk2(a[d],a[d+1]);
}

#define K3_ASZ 16384
#define K3_BSZ 20480
#define K3_STG (K3_ASZ+K3_BSZ)
#define K3_NS 4
__global__ __launch_bounds__(512,2) void k3(const u16* __restrict__ x, const u16* __restrict__ o, const u16* __restrict__ h,
    const u16* __restrict__ wo, const u16* __restrict__ wd, u16* __restrict__ out){
  __shared__ __attribute__((aligned(16))) char smem[K3_NS*K3_STG];
  const int tid=threadIdx.x, lane=tid&63, wid=tid>>6;
  const int nt = blockIdx.x >> 5, mt = (blockIdx.x&7)*4 + ((blockIdx.x>>3)&3);
  const int m0=mt*64, n0=nt*80;
  int aoff[2];
  #pragma unroll
  for(int i=0;i<2;i++){ int id=wid*2+i; int hf=id>>3; int r=(id&7)*8+(lane>>3); aoff[i]=(r<<16) | (hf*64 + ((lane&7)^((r>>1)&7))*8); }
  int boff[3];
  #pragma unroll
  for(int i=0;i<3;i++){ int idx = wid + 8*i; int L=(idx*64+lane)*16; boff[i]=(L/160)*D_ + n0 + (L%160)/2; }
  auto issue=[&](int kt, char* buf){
    const u16* A; int lda, k0; const u16* B;
    if(kt<5){ A=o; lda=D_; k0=kt*128; B=wo+(size_t)k0*D_; } else { A=h; lda=F_; k0=(kt-5)*128; B=wd+(size_t)k0*D_; }
    const bool last = (kt==18);
    #pragma unroll
    for(int i=0;i<2;i++){
      int koff = aoff[i]&0xffff; if(last && koff>=64) koff-=64;
      glds16((const void*)(A+(size_t)(m0+(aoff[i]>>16))*lda+k0+koff), LADDR(buf+(wid*2+i)*1024));
    }
    #pragma unroll
    for(int i=0;i<3;i++){ if(wid+8*i<20){
      int bo=boff[i]; if(last && (wid+8*i)>=10) bo-=64*D_;
      glds16((const void*)(B+bo), LADDR(buf+K3_ASZ+(wid+8*i)*1024)); } }
  };
  f32x4 acc[2][5];
  #pragma unroll
  for(int a=0;a<2;a++)
    #pragma unroll
    for(int b=0;b<5;b++) acc[a][b]=(f32x4){0,0,0,0};
  const int wr=wid&1, ks=wid>>1;
  const int g=lane>>4, l15=lane&15, q=(lane&15)>>2, p=lane&3;
  issue(0,smem); issue(1,smem+K3_STG); issue(2,smem+2*K3_STG);
  #pragma unroll
  for(int kt=0;kt<19;++kt){
    char* cur=smem+(kt%K3_NS)*K3_STG;
    if(kt<17){ if(wid<4) asm volatile("s_waitcnt vmcnt(10)":::"memory"); else asm volatile("s_waitcnt vmcnt(8)":::"memory"); }
    else if(kt==17){ if(wid<4) asm volatile("s_waitcnt vmcnt(5)":::"memory"); else asm volatile("s_waitcnt vmcnt(4)":::"memory"); }
    else asm volatile("s_waitcnt vmcnt(0)":::"memory");
    __builtin_amdgcn_s_barrier();
    if(kt+3<19) issue(kt+3, smem+((kt+3)%K3_NS)*K3_STG);
    if(kt<18 || ks<2){
      s16x8 af[2];
      #pragma unroll
      for(int a=0;a<2;a++){ int row=wr*32+a*16+l15; int ch=(ks&1)*4+g; af[a]=*(const s16x8*)(cur+(ks>>1)*8192+row*128+((ch^((row>>1)&7))*16)); }
      #pragma unroll
      for(int b=0;b<5;b++){
        int row=ks*32+8*g+q;
        s16x4 t0=tr_read(cur+K3_ASZ+row*160+b*32+p*8);
        s16x4 t1=tr_read(cur+K3_ASZ+(row+4)*160+b*32+p*8);
        s16x8 bf=__builtin_shufflevector(t0,t1,0,1,2,3,4,5,6,7);
        #pragma unroll
        for(int a=0;a<2;a++) acc[a][b]=__builtin_amdgcn_mfma_f32_16x16x32_bf16(bf,af[a],acc[a][b],0,0,0);
      }
    }
    asm volatile("s_waitcnt lgkmcnt(0)":::"memory");
  }
  __syncthreads();
  float* red=(float*)smem;
  if(ks>0){
    #pragma unroll
    for(int a=0;a<2;a++)
      #pragma unroll
      for(int b=0;b<5;b++) *(f32x4*)(red + ((((ks-1)*2+wr)*10+a*5+b)*64+lane)*4)=acc[a][b];
  }
  __syncthreads();
  if(ks==0){
    #pragma unroll
    for(int a=0;a<2;a++){
      int m=m0+wr*32+a*16+l15;
      #pragma unroll
      for(int b=0;b<5;b++){
        f32x4 r=acc[a][b];
        #pragma unroll
        for(int z=0;z<3;z++) r+=*(const f32x4*)(red + (((z*2+wr)*10+a*5+b)*64+lane)*4);
        int n=n0+b*16+g*4;
        u32x2 xv=*(const u32x2*)(x+(size_t)m*D_+n);
        u32x2 ov; ov[0]=pk2(lo16(xv[0])+r[0], hi16(xv[0])+r[1]);
        ov[1]=pk2(lo16(xv[1])+r[2], hi16(xv[1])+r[3]);
        *(u32x2*)(out+(size_t)m*D_+n)=ov;
      }
    }
  }
}

extern "C" void kernel_launch(void* const* d_in, const int* in_sizes, int n_in,
                              void* d_out, int out_size, void* d_ws, size_t ws_size,
                              hipStream_t stream) {
  const u16* x=(const u16*)d_in[0]; const u16* wqkv=(const u16*)d_in[1]; const u16* wo=(const u16*)d_in[2];
  const u16* wg=(const u16*)d_in[4]; const u16* wu=(const u16*)d_in[5]; const u16* wd=(const u16*)d_in[6];
  u16* ws=(u16*)d_ws;
  u16* qkv=ws; u16* h=qkv+S_*1920; u16* o=h+S_*F_; u16* xn=o+S_*D_;
  k1<<<8*28,512,0,stream>>>(x,wqkv,wg,wu,qkv,h);
  attn_kernel<<<S_/8,256,0,stream>>>(qkv,o);
  k3<<<256,512,0,stream>>>(x,o,h,wo,wd,(u16*)d_out);
}
```

```cpp
#include <hip/hip_runtime.h>
#include <hip/hip_bf16.h>
#include <stdint.h>

typedef unsigned short u16;
typedef short s16x8 __attribute__((ext_vector_type(8)));
typedef short s16x4 __attribute__((ext_vector_type(4)));
typedef float f32x4 __attribute__((ext_vector_type(4)));
typedef unsigned u32x4 __attribute__((ext_vector_type(4)));
typedef unsigned u32x2 __attribute__((ext_vector_type(2)));
#define LDS_PTR(T) __attribute__((address_space(3))) T*

#define S_ 2048
#define D_ 640
#define F_ 1728
#define DH 20

__device__ __forceinline__ float bf2f(u16 v){ return __uint_as_float(((unsigned)v)<<16); }
__device__ __forceinline__ u16 f2bf(float f){ __hip_bfloat16 b = __float2bfloat16(f); return *(u16*)&b; }
__device__ __forceinline__ float rbf(float f){ return bf2f(f2bf(f)); }
__device__ __forceinline__ unsigned pk2(float a, float b){ return (unsigned)f2bf(a) | ((unsigned)f2bf(b)<<16); }
__device__ __forceinline__ float lo16(unsigned v){ return __uint_as_float(v<<16); }
__device__ __forceinline__ float hi16(unsigned v){ return __uint_as_float(v&0xffff0000u); }

__device__ __forceinline__ s16x4 tr_read(const char* p){
  return __builtin_amdgcn_ds_read_tr16_b64_v4i16((LDS_PTR(s16x4))p);
}


__device__ __forceinline__ void glds16(const void* gsrc, unsigned lds_dst){
  unsigned keep;
  asm volatile("s_mov_b32 %0, m0\n\ts_mov_b32 m0, %2\n\ts_nop 0\n\tglobal_load_lds_dwordx4 %1, off\n\ts_mov_b32 m0, %0" : "=&s"(keep) : "v"(gsrc), "s"(lds_dst) : "memory");
}
#define LADDR(p) ((unsigned)__builtin_amdgcn_readfirstlane((int)(unsigned)(size_t)(p)))
__global__ __launch_bounds__(256) void ln_kernel(const u16* __restrict__ x, u16* __restrict__ xn){
  int lane=threadIdx.x&63; int row=blockIdx.x*16+(threadIdx.x>>6)*4+(lane>>4); int l16=lane&15;
  const u16* xr=x+(size_t)row*D_;
  float v[40]; float s=0;
  #pragma unroll
  for(int i=0;i<5;i++){ u32x4 d=*(const u32x4*)(xr+(l16+16*i)*8);
    #pragma unroll
    for(int j=0;j<4;j++){ v[i*8+2*j]=lo16(d[j]); v[i*8+2*j+1]=hi16(d[j]); } }
  #pragma unroll
  for(int i=0;i<40;i++) s+=v[i];
  #pragma unroll
  for(int o=8;o>0;o>>=1) s+=__shfl_xor(s,o);
  float mean=s*(1.0f/D_); float q=0;
  #pragma unroll
  for(int i=0;i<40;i++){ float d=v[i]-mean; q+=d*d; }
  #pragma unroll
  for(int o=8;o>0;o>>=1) q+=__shfl_xor(q,o);
  float mb=rbf(mean), vb=rbf(q*(1.0f/D_));
  float ri=1.0f/rbf(sqrtf(rbf(vb+1e-5f)));
  #pragma unroll
  for(int i=0;i<5;i++){ u32x4 o;
    #pragma unroll
    for(int j=0;j<4;j++) o[j]=pk2(rbf(v[i*8+2*j]-mb)*ri, rbf(v[i*8+2*j+1]-mb)*ri);
    *(u32x4*)(xn+(size_t)row*D_+(l16+16*i)*8)=o; }
}
#define K1_ASZ 32768
#define K1_STG 65536
template<bool ISQ>
__device__ __forceinline__ void k1_body(char* smem, const u16* __restrict__ xn, const u16* __restrict__ wqkv,
    const u16* __restrict__ wg, const u16* __restrict__ wu, u16* __restrict__ o, u16* __restrict__ h, int mt, int nt){
  constexpr int NB = ISQ?8:6;
  constexpr int BROW = ISQ?512:384;
  constexpr int NBI = ISQ?4:3;
  const int tid=threadIdx.x, lane=tid&63, wid=tid>>6;
  const int m0 = ISQ? mt*240-16 : mt*256;
  const u16* bsrc[NBI]; constexpr int ldb = ISQ?1920:F_;
  #pragma unroll
  for(int i=0;i<NBI;i++){
    int L = ((wid*NBI+i)*64+lane)*16;
    int row = L/BROW, pc=(L%BROW)/16;
    const u16* p;
    if(ISQ){
      int sw=(row&3)|(((row>>3)&1)<<2);
      int col=((pc>>1)^sw)*16+(pc&1)*8;
      if(col>=240) col-=240;
      p = wqkv + (col/80)*640 + nt*80 + col%80;
    } else {
      int sw = ((row>>1)&1) | (((row>>3)&1)<<1);
      int col = ((pc>>1)^sw)*16 + (pc&1)*8;
      int wc=col/96, cc=col%96; p = (cc<48)? (wg + nt*96+wc*48+cc) : (wu + nt*96+wc*48+cc-48);
    }
    bsrc[i] = p + (size_t)row*ldb;
  }
  const u16* asrc[4];
  #pragma unroll
  for(int i=0;i<4;i++){ int r=(wid*4+i)*8+(lane>>3); int gr=m0+r; gr=gr<0?0:(gr>S_-1?S_-1:gr); asrc[i]=xn+(size_t)gr*D_+(((lane&7)^((r>>1)&7))*8); }
  auto issue=[&](int kt, char* buf){
    #pragma unroll
    for(int i=0;i<NBI;i++)
      glds16((const void*)(bsrc[i] + (size_t)kt*64*ldb), LADDR(buf + K1_ASZ + (wid*NBI+i)*1024));
    #pragma unroll
    for(int i=0;i<4;i++)
      glds16((const void*)(asrc[i]+kt*64), LADDR(buf+(wid*4+i)*1024));
  };
  f32x4 acc[4][NB];
  #pragma unroll
  for(int a=0;a<4;a++)
    #pragma unroll
    for(int b=0;b<NB;b++) acc[a][b]=(f32x4){0,0,0,0};
  const int wr=wid>>1, wc=wid&1;
  const int g=lane>>4, l15=lane&15, q=(lane&15)>>2, p=lane&3;
  issue(0,smem);
  #pragma unroll
  for(int kt=0; kt<10; ++kt){
    char* cur = smem + (kt&1)*K1_STG;
    asm volatile("s_waitcnt vmcnt(0)":::"memory");
    __builtin_amdgcn_s_barrier();
    if(kt+1<10) issue(kt+1, smem+((kt+1)&1)*K1_STG);
    #pragma unroll
    for(int ks=0; ks<2; ++ks){
      s16x8 af[4];
      #pragma unroll
      for(int a=0;a<4;a++){
        int row = wr*64 + a*16 + l15;
        int ch = ks*4+g;
        af[a] = *(const s16x8*)(cur + row*128 + ((ch^((row>>1)&7))*16));
      }
      #pragma unroll
      for(int b=0;b<NB;b++){
        int c = wc*NB+b;
        s16x4 t0,t1;
        { int row=ks*32+8*g+q; int sw=ISQ?((row&3)|(((row>>3)&1)<<2)):(((row>>1)&1)|(((row>>3)&1)<<1));
          t0=tr_read(cur+K1_ASZ+row*BROW+((c^sw)*32)+p*8); }
        { int row=ks*32+8*g+4+q; int sw=ISQ?((row&3)|(((row>>3)&1)<<2)):(((row>>1)&1)|(((row>>3)&1)<<1));
          t1=tr_read(cur+K1_ASZ+row*BROW+((c^sw)*32)+p*8); }
        s16x8 bf = __builtin_shufflevector(t0,t1,0,1,2,3,4,5,6,7);
        #pragma unroll
        for(int a=0;a<4;a++)
          acc[a][b]=__builtin_amdgcn_mfma_f32_16x16x32_bf16(bf, af[a], acc[a][b],0,0,0);
      }
    }
    asm volatile("s_waitcnt lgkmcnt(0)":::"memory");
  }
  __builtin_amdgcn_s_barrier();
  if(ISQ){
    #pragma unroll
    for(int a=0;a<4;a++)
      #pragma unroll
      for(int b=0;b<NB;b++){
        int col=(wc*8+b)*16;
        if(col<240){
          u32x2 ov; ov[0]=pk2(acc[a][b][0],acc[a][b][1]); ov[1]=pk2(acc[a][b][2],acc[a][b][3]);
          *(u32x2*)(smem + (wr*64+a*16+l15)*496 + col*2 + g*8) = ov;
        }
      }
    __syncthreads();
    #pragma unroll
    for(int i=0;i<2;i++){
      int pair=tid+512*i;
      int r=16+(pair>>2), hh=pair&3; int sg=m0+r;
      if(pair<960 && sg<S_){
        float qv[DH];
        const char* qp=smem+r*496+hh*40;
        #pragma unroll
        for(int d=0;d<5;d++){ u32x2 t=*(const u32x2*)(qp+d*8); qv[d*4]=lo16(t[0]); qv[d*4+1]=hi16(t[0]); qv[d*4+2]=lo16(t[1]); qv[d*4+3]=hi16(t[1]); }
        float lg[8];
        #pragma unroll
        for(int w=0;w<8;w++){
          const char* kp=smem+(r-2*w)*496+160+hh*40; float aa=0;
          #pragma unroll
          for(int d=0;d<5;d++){ u32x2 t=*(const u32x2*)(kp+d*8); aa+=qv[d*4]*lo16(t[0]); aa+=qv[d*4+1]*hi16(t[0]); aa+=qv[d*4+2]*lo16(t[1]); aa+=qv[d*4+3]*hi16(t[1]); }
          lg[w]=(sg-2*w>=0)? rbf(rbf(aa)*0.2236328125f) : -1e9f;
        }
        float mx=lg[0];
        #pragma unroll
        for(int w=1;w<8;w++) mx=fmaxf(mx,lg[w]);
        float e[8], sum=0;
        #pragma unroll
        for(int w=0;w<8;w++){ e[w]=__expf(lg[w]-mx); sum+=e[w]; }
        #pragma unroll
        for(int w=0;w<8;w++) e[w]=rbf(e[w]/sum);
        float oa[DH];
        #pragma unroll
        for(int d=0;d<DH;d++) oa[d]=0;
        #pragma unroll
        for(int w=0;w<8;w++){
          const char* vp=smem+(r-2*w)*496+320+hh*40;
          #pragma unroll
          for(int d=0;d<5;d++){ u32x2 t=*(const u32x2*)(vp+d*8); oa[d*4]+=e[w]*lo16(t[0]); oa[d*4+1]+=e[w]*hi16(t[0]); oa[d*4+2]+=e[w]*lo16(t[1]); oa[d*4+3]+=e[w]*hi16(t[1]); }
        }
        u16* op=o+(size_t)sg*D_+nt*80+hh*20;
        #pragma unroll
        for(int d=0;d<5;d++){ u32x2 t; t[0]=pk2(oa[d*4],oa[d*4+1]); t[1]=pk2(oa[d*4+2],oa[d*4+3]); *(u32x2*)(op+d*4)=t; }
      }
    }
  } else {
    char* eb = smem + wid*(64*208);
    #pragma unroll
    for(int a=0;a<4;a++)
      #pragma unroll
      for(int b=0;b<3;b++){
        float r[4];
        #pragma unroll
        for(int e=0;e<4;e++){ float gv=rbf(acc[a][b][e]); gv=gv>0.f?gv:0.f; r[e]=gv*rbf(acc[a][(b+3)%NB][e]); }
        u32x2 ov; ov[0]=pk2(r[0],r[1]); ov[1]=pk2(r[2],r[3]);
        *(u32x2*)(eb + (a*16+l15)*208 + b*32 + g*8) = ov;
      }
    __builtin_amdgcn_s_waitcnt(0); __builtin_amdgcn_wave_barrier();
    #pragma unroll
    for(int i=0;i<6;i++){ int id=i*64+lane; int r=id/6, c=id%6;
      u32x4 v=*(const u32x4*)(eb + r*208 + c*16);
      *(u32x4*)(h + (size_t)(m0+wr*64+r)*F_ + nt*96 + wc*48 + c*8)=v; }
  }
}
__global__ __launch_bounds__(512,2) void k1(const u16* __restrict__ xn, const u16* __restrict__ wqkv,
    const u16* __restrict__ wg, const u16* __restrict__ wu, u16* __restrict__ o, u16* __restrict__ h){
  __shared__ __attribute__((aligned(16))) char smem[2*K1_STG];
  const int xcd=blockIdx.x&7, idx=blockIdx.x>>3;
  if(idx<9) k1_body<true>(smem,xn,wqkv,wg,wu,o,h,idx,xcd);
  else k1_body<false>(smem,xn,wqkv,wg,wu,o,h,xcd,idx-9);
}

__global__ __launch_bounds__(256) void attn_kernel(const u16* __restrict__ qkv, u16* __restrict__ o){
  int s = blockIdx.x*8 + (threadIdx.x>>5); int h = threadIdx.x&31;
  const u16* qp = qkv + (size_t)s*1920 + h*DH;
  float qv[DH];
  #pragma unroll
  for(int d=0;d<DH;d+=2){ unsigned t=*(const unsigned*)(qp+d); qv[d]=lo16(t); qv[d+1]=hi16(t); }
  float lg[8]; const float scale = 0.2236328125f;
  #pragma unroll
  for(int w=0;w<8;w++){
    int p = s-2*w;
    if(p>=0){
      const u16* k = qkv + (size_t)p*1920 + 640 + h*DH;
      float a=0;
      #pragma unroll
      for(int d=0;d<DH;d+=2){ unsigned t=*(const unsigned*)(k+d); a+=qv[d]*lo16(t); a+=qv[d+1]*hi16(t); }
      lg[w]=rbf(rbf(a)*scale);
    } else lg[w]=-1e9f;
  }
  float mx=lg[0];
  #pragma unroll
  for(int w=1;w<8;w++) mx=fmaxf(mx,lg[w]);
  float e[8]; float sum=0;
  #pragma unroll
  for(int w=0;w<8;w++){ e[w]=__expf(lg[w]-mx); sum+=e[w]; }
  #pragma unroll
  for(int w=0;w<8;w++) e[w]=rbf(e[w]/sum);
  float a[DH];
  #pragma unroll
  for(int d=0;d<DH;d++) a[d]=0;
  #pragma unroll
  for(int w=0;w<8;w++){ int p=s-2*w; if(p<0)p=0; const u16* v=qkv+(size_t)p*1920+1280+h*DH;
    #pragma unroll
    for(int d=0;d<DH;d+=2){ unsigned t=*(const unsigned*)(v+d); a[d]+=e[w]*lo16(t); a[d+1]+=e[w]*hi16(t); } }
  #pragma unroll
  for(int d=0;d<DH;d+=2) *(unsigned*)(o+(size_t)s*D_+h*DH+d)=pk2(a[d],a[d+1]);
}

#define K3_ASZ 16384
#define K3_BSZ 20480
#define K3_STG (K3_ASZ+K3_BSZ)
#define K3_NS 4
__global__ __launch_bounds__(512,2) void k3(const u16* __restrict__ x, const u16* __restrict__ o, const u16* __restrict__ h,
    const u16* __restrict__ wo, const u16* __restrict__ wd, u16* __restrict__ out){
  __shared__ __attribute__((aligned(16))) char smem[K3_NS*K3_STG];
  const int tid=threadIdx.x, lane=tid&63, wid=tid>>6;
  const int nt = blockIdx.x >> 5, mt = (blockIdx.x&7)*4 + ((blockIdx.x>>3)&3);
  const int m0=mt*64, n0=nt*80;
  int aoff[2];
  #pragma unroll
  for(int i=0;i<2;i++){ int id=wid*2+i; int hf=id>>3; int r=(id&7)*8+(lane>>3); aoff[i]=(r<<16) | (hf*64 + ((lane&7)^((r>>1)&7))*8); }
  int boff[3];
  #pragma unroll
  for(int i=0;i<3;i++){ int idx = wid + 8*i; int L=(idx*64+lane)*16; boff[i]=(L/160)*D_ + n0 + (L%160)/2; }
  auto issue=[&](int kt, char* buf){
    const u16* A; int lda, k0; const u16* B;
    if(kt<5){ A=o; lda=D_; k0=kt*128; B=wo+(size_t)k0*D_; } else { A=h; lda=F_; k0=(kt-5)*128; B=wd+(size_t)k0*D_; }
    const bool last = (kt==18);
    #pragma unroll
    for(int i=0;i<2;i++){
      int koff = aoff[i]&0xffff; if(last && koff>=64) koff-=64;
      glds16((const void*)(A+(size_t)(m0+(aoff[i]>>16))*lda+k0+koff), LADDR(buf+(wid*2+i)*1024));
    }
    #pragma unroll
    for(int i=0;i<3;i++){ if(wid+8*i<20){
      int bo=boff[i]; if(last && (wid+8*i)>=10) bo-=64*D_;
      glds16((const void*)(B+bo), LADDR(buf+K3_ASZ+(wid+8*i)*1024)); } }
  };
  f32x4 acc[2][5];
  #pragma unroll
  for(int a=0;a<2;a++)
    #pragma unroll
    for(int b=0;b<5;b++) acc[a][b]=(f32x4){0,0,0,0};
  const int wr=wid&1, ks=wid>>1;
  const int g=lane>>4, l15=lane&15, q=(lane&15)>>2, p=lane&3;
  issue(0,smem); issue(1,smem+K3_STG); issue(2,smem+2*K3_STG);
  #pragma unroll
  for(int kt=0;kt<19;++kt){
    char* cur=smem+(kt%K3_NS)*K3_STG;
    if(kt<17){ if(wid<4) asm volatile("s_waitcnt vmcnt(10)":::"memory"); else asm volatile("s_waitcnt vmcnt(8)":::"memory"); }
    else if(kt==17){ if(wid<4) asm volatile("s_waitcnt vmcnt(5)":::"memory"); else asm volatile("s_waitcnt vmcnt(4)":::"memory"); }
    else asm volatile("s_waitcnt vmcnt(0)":::"memory");
    __builtin_amdgcn_s_barrier();
    if(kt+3<19) issue(kt+3, smem+((kt+3)%K3_NS)*K3_STG);
    if(kt<18 || ks<2){
      s16x8 af[2];
      #pragma unroll
      for(int a=0;a<2;a++){ int row=wr*32+a*16+l15; int ch=(ks&1)*4+g; af[a]=*(const s16x8*)(cur+(ks>>1)*8192+row*128+((ch^((row>>1)&7))*16)); }
      #pragma unroll
      for(int b=0;b<5;b++){
        int row=ks*32+8*g+q;
        s16x4 t0=tr_read(cur+K3_ASZ+row*160+b*32+p*8);
        s16x4 t1=tr_read(cur+K3_ASZ+(row+4)*160+b*32+p*8);
        s16x8 bf=__builtin_shufflevector(t0,t1,0,1,2,3,4,5,6,7);
        #pragma unroll
        for(int a=0;a<2;a++) acc[a][b]=__builtin_amdgcn_mfma_f32_16x16x32_bf16(bf,af[a],acc[a][b],0,0,0);
      }
    }
    asm volatile("s_waitcnt lgkmcnt(0)":::"memory");
  }
  __syncthreads();
  float* red=(float*)smem;
  if(ks>0){
    #pragma unroll
    for(int a=0;a<2;a++)
      #pragma unroll
      for(int b=0;b<5;b++) *(f32x4*)(red + ((((ks-1)*2+wr)*10+a*5+b)*64+lane)*4)=acc[a][b];
  }
  __syncthreads();
  if(ks==0){
    #pragma unroll
    for(int a=0;a<2;a++){
      int m=m0+wr*32+a*16+l15;
      #pragma unroll
      for(int b=0;b<5;b++){
        f32x4 r=acc[a][b];
        #pragma unroll
        for(int z=0;z<3;z++) r+=*(const f32x4*)(red + (((z*2+wr)*10+a*5+b)*64+lane)*4);
        int n=n0+b*16+g*4;
        u32x2 xv=*(const u32x2*)(x+(size_t)m*D_+n);
        u32x2 ov; ov[0]=pk2(lo16(xv[0])+r[0], hi16(xv[0])+r[1]);
        ov[1]=pk2(lo16(xv[1])+r[2], hi16(xv[1])+r[3]);
        *(u32x2*)(out+(size_t)m*D_+n)=ov;
      }
    }
  }
}

extern "C" void kernel_launch(void* const* d_in, const int* in_sizes, int n_in,
                              void* d_out, int out_size, void* d_ws, size_t ws_size,
                              hipStream_t stream) {
  const u16* x=(const u16*)d_in[0]; const u16* wqkv=(const u16*)d_in[1]; const u16* wo=(const u16*)d_in[2];
  const u16* wg=(const u16*)d_in[4]; const u16* wu=(const u16*)d_in[5]; const u16* wd=(const u16*)d_in[6];
  u16* ws=(u16*)d_ws;
  u16* qkv=ws; u16* h=qkv+S_*1920; u16* o=h+S_*F_; u16* xn=o+S_*D_;
  ln_kernel<<<128,256,0,stream>>>(x,xn);
  k1<<<216,512,0,stream>>>(xn,wqkv,wg,wu,o,h);
  k3<<<256,512,0,stream>>>(x,o,h,wo,wd,(u16*)d_out);
}
```

```cpp
#include <hip/hip_runtime.h>
#include <hip/hip_bf16.h>
#include <stdint.h>

typedef unsigned short u16;
typedef short s16x8 __attribute__((ext_vector_type(8)));
typedef short s16x4 __attribute__((ext_vector_type(4)));
typedef float f32x4 __attribute__((ext_vector_type(4)));
typedef unsigned u32x4 __attribute__((ext_vector_type(4)));
typedef unsigned u32x2 __attribute__((ext_vector_type(2)));
#define LDS_PTR(T) __attribute__((address_space(3))) T*

#define S_ 2048
#define D_ 640
#define F_ 1728
#define DH 20

__device__ __forceinline__ float bf2f(u16 v){ return __uint_as_float(((unsigned)v)<<16); }
__device__ __forceinline__ u16 f2bf(float f){ __hip_bfloat16 b = __float2bfloat16(f); return *(u16*)&b; }
__device__ __forceinline__ float rbf(float f){ return bf2f(f2bf(f)); }
__device__ __forceinline__ unsigned pk2(float a, float b){ return (unsigned)f2bf(a) | ((unsigned)f2bf(b)<<16); }
__device__ __forceinline__ float lo16(unsigned v){ return __uint_as_float(v<<16); }
__device__ __forceinline__ float hi16(unsigned v){ return __uint_as_float(v&0xffff0000u); }

__device__ __forceinline__ s16x4 tr_read(const char* p){
  return __builtin_amdgcn_ds_read_tr16_b64_v4i16((LDS_PTR(s16x4))p);
}

__device__ __forceinline__ void glds16(const void* gsrc, unsigned lds_dst){
  unsigned keep;
  asm volatile("s_mov_b32 %0, m0\n\ts_mov_b32 m0, %2\n\ts_nop 0\n\tglobal_load_lds_dwordx4 %1, off\n\ts_mov_b32 m0, %0" : "=&s"(keep) : "v"(gsrc), "s"(lds_dst) : "memory");
}
#define LADDR(p) ((unsigned)__builtin_amdgcn_readfirstlane((int)(unsigned)(size_t)(p)))
__global__ __launch_bounds__(256) void ln_kernel(const u16* __restrict__ x, u16* __restrict__ xn){
  int lane=threadIdx.x&63; int row=blockIdx.x*16+(threadIdx.x>>6)*4+(lane>>4); int l16=lane&15;
  const u16* xr=x+(size_t)row*D_;
  float v[40]; float s=0;
  #pragma unroll
  for(int i=0;i<5;i++){ u32x4 d=*(const u32x4*)(xr+(l16+16*i)*8);
    #pragma unroll
    for(int j=0;j<4;j++){ v[i*8+2*j]=lo16(d[j]); v[i*8+2*j+1]=hi16(d[j]); } }
  #pragma unroll
  for(int i=0;i<40;i++) s+=v[i];
  #pragma unroll
  for(int o=8;o>0;o>>=1) s+=__shfl_xor(s,o);
  float mean=s*(1.0f/D_); float q=0;
  #pragma unroll
  for(int i=0;i<40;i++){ float d=v[i]-mean; q+=d*d; }
  #pragma unroll
  for(int o=8;o>0;o>>=1) q+=__shfl_xor(q,o);
  float mb=rbf(mean), vb=rbf(q*(1.0f/D_));
  float ri=1.0f/rbf(sqrtf(rbf(vb+1e-5f)));
  #pragma unroll
  for(int i=0;i<5;i++){ u32x4 o;
    #pragma unroll
    for(int j=0;j<4;j++) o[j]=pk2(rbf(v[i*8+2*j]-mb)*ri, rbf(v[i*8+2*j+1]-mb)*ri);
    *(u32x4*)(xn+(size_t)row*D_+(l16+16*i)*8)=o; }
}
#define K1_ASZ 16384
#define K1_BSZ 12288
#define K1_STG (K1_ASZ+K1_BSZ)
#define K1_NS 5
__global__ __launch_bounds__(512,2) void k1(const u16* __restrict__ xn, const u16* __restrict__ wqkv,
    const u16* __restrict__ wg, const u16* __restrict__ wu, u16* __restrict__ qkv, u16* __restrict__ h){
  __shared__ __attribute__((aligned(16))) char smem[K1_NS*K1_STG];
  const int tid=threadIdx.x, lane=tid&63, wid=tid>>6;
  const int bid=blockIdx.x;
  const int xcd=bid&7, idx=bid>>3;
  const int mt=(xcd&1)*4+(idx&3), nt=(xcd>>1)*7+(idx>>2);
  const int m0 = mt*256;
  const bool isq = nt<10;
  const u16* bsrc[2]; int ldb = isq?1920:F_;
  #pragma unroll
  for(int i=0;i<2;i++){
    int id = wid+8*i; if(id>=12) id=wid;
    int L = (id*64+lane)*16;
    int row = L/384, pc=(L%384)/16;
    int sw = ((row>>1)&1) | (((row>>3)&1)<<1);
    int c = (pc>>1)^sw;
    int col = c*16 + (pc&1)*8;
    const u16* p;
    if(isq) p = wqkv + nt*192 + col;
    else { int j=nt-10; int wc=col/96, cc=col%96; p = (cc<48)? (wg + j*96+wc*48+cc) : (wu + j*96+wc*48+cc-48); }
    bsrc[i] = p + (size_t)row*ldb;
  }
  const u16* asrc[2];
  #pragma unroll
  for(int i=0;i<2;i++){ int r=(wid*2+i)*16+(lane>>2); asrc[i]=xn+(size_t)(m0+r)*D_+(((lane&3)^((4-((r>>2)&3))&3))*8); }
  auto issue=[&](int kt, char* buf){
    glds16((const void*)(bsrc[0] + (size_t)kt*32*ldb), LADDR(buf + K1_ASZ + wid*1024));
    if(wid<4) glds16((const void*)(bsrc[1] + (size_t)kt*32*ldb), LADDR(buf + K1_ASZ + (wid+8)*1024));
    #pragma unroll
    for(int i=0;i<2;i++)
      glds16((const void*)(asrc[i]+kt*32), LADDR(buf+(wid*2+i)*1024));
  };
  f32x4 acc[4][6];
  #pragma unroll
  for(int a=0;a<4;a++)
    #pragma unroll
    for(int b=0;b<6;b++) acc[a][b]=(f32x4){0,0,0,0};
  const int wr=wid>>1, wc=wid&1;
  const int g=lane>>4, l15=lane&15, q=(lane&15)>>2, p=lane&3;
  #pragma unroll
  for(int i=0;i<4;i++) issue(i,smem+i*K1_STG);
  #pragma unroll
  for(int kt=0; kt<20; ++kt){
    char* cur = smem + (kt%K1_NS)*K1_STG;
    if(wid<4){ if(kt<17) asm volatile("s_waitcnt vmcnt(12)":::"memory"); else if(kt==17) asm volatile("s_waitcnt vmcnt(8)":::"memory"); else if(kt==18) asm volatile("s_waitcnt vmcnt(4)":::"memory"); else asm volatile("s_waitcnt vmcnt(0)":::"memory"); }
    else { if(kt<17) asm volatile("s_waitcnt vmcnt(9)":::"memory"); else if(kt==17) asm volatile("s_waitcnt vmcnt(6)":::"memory"); else if(kt==18) asm volatile("s_waitcnt vmcnt(3)":::"memory"); else asm volatile("s_waitcnt vmcnt(0)":::"memory"); }
    __builtin_amdgcn_s_barrier();
    if(kt+4<20) issue(kt+4, smem+((kt+4)%K1_NS)*K1_STG);
    {
      s16x8 af[4];
      #pragma unroll
      for(int a=0;a<4;a++){
        int row = wr*64 + a*16 + l15;
        af[a] = *(const s16x8*)(cur + row*64 + ((g^((4-((row>>2)&3))&3))*16));
      }
      #pragma unroll
      for(int b=0;b<6;b++){
        int c = wc*6+b;
        s16x4 t0,t1;
        { int row=8*g+q; int sw=((row>>1)&1)|(((row>>3)&1)<<1);
          t0=tr_read(cur+K1_ASZ+row*384+((c^sw)*32)+p*8); }
        { int row=8*g+4+q; int sw=((row>>1)&1)|(((row>>3)&1)<<1);
          t1=tr_read(cur+K1_ASZ+row*384+((c^sw)*32)+p*8); }
        s16x8 bf = __builtin_shufflevector(t0,t1,0,1,2,3,4,5,6,7);
        #pragma unroll
        for(int a=0;a<4;a++)
          acc[a][b]=__builtin_amdgcn_mfma_f32_16x16x32_bf16(bf, af[a], acc[a][b],0,0,0);
      }
    }
    asm volatile("s_waitcnt lgkmcnt(0)":::"memory");
  }
  __builtin_amdgcn_s_barrier();
  char* eb = smem + wid*(64*208);
  if(isq){
    #pragma unroll
    for(int a=0;a<4;a++)
      #pragma unroll
      for(int b=0;b<6;b++){
        u32x2 o; o[0]=pk2(acc[a][b][0],acc[a][b][1]); o[1]=pk2(acc[a][b][2],acc[a][b][3]);
        *(u32x2*)(eb + (a*16+l15)*208 + b*32 + g*8) = o;
      }
    __builtin_amdgcn_s_waitcnt(0); __builtin_amdgcn_wave_barrier();
    #pragma unroll
    for(int i=0;i<12;i++){ int id=i*64+lane; int r=id/12, c=id%12;
      u32x4 v=*(const u32x4*)(eb + r*208 + c*16);
      *(u32x4*)(qkv + (size_t)(m0+wr*64+r)*1920 + nt*192 + wc*96 + c*8)=v; }
  } else {
    int j=nt-10;
    #pragma unroll
    for(int a=0;a<4;a++)
      #pragma unroll
      for(int b=0;b<3;b++){
        float r[4];
        #pragma unroll
        for(int e=0;e<4;e++){ float gv=rbf(acc[a][b][e]); gv=gv>0.f?gv:0.f; r[e]=gv*rbf(acc[a][b+3][e]); }
        u32x2 o; o[0]=pk2(r[0],r[1]); o[1]=pk2(r[2],r[3]);
        *(u32x2*)(eb + (a*16+l15)*208 + b*32 + g*8) = o;
      }
    __builtin_amdgcn_s_waitcnt(0); __builtin_amdgcn_wave_barrier();
    #pragma unroll
    for(int i=0;i<6;i++){ int id=i*64+lane; int r=id/6, c=id%6;
      u32x4 v=*(const u32x4*)(eb + r*208 + c*16);
      *(u32x4*)(h + (size_t)(m0+wr*64+r)*F_ + j*96 + wc*48 + c*8)=v; }
  }
}

__global__ __launch_bounds__(256) void attn_kernel(const u16* __restrict__ qkv, u16* __restrict__ o){
  int s = blockIdx.x*8 + (threadIdx.x>>5); int h = threadIdx.x&31;
  const u16* qp = qkv + (size_t)s*1920 + h*DH;
  float qv[DH];
  #pragma unroll
  for(int d=0;d<DH;d+=2){ unsigned t=*(const unsigned*)(qp+d); qv[d]=lo16(t); qv[d+1]=hi16(t); }
  float lg[8]; const float scale = 0.2236328125f;
  #pragma unroll
  for(int w=0;w<8;w++){
    int p = s-2*w;
    if(p>=0){
      const u16* k = qkv + (size_t)p*1920 + 640 + h*DH;
      float a=0;
      #pragma unroll
      for(int d=0;d<DH;d+=2){ unsigned t=*(const unsigned*)(k+d); a+=qv[d]*lo16(t); a+=qv[d+1]*hi16(t); }
      lg[w]=rbf(rbf(a)*scale);
    } else lg[w]=-1e9f;
  }
  float mx=lg[0];
  #pragma unroll
  for(int w=1;w<8;w++) mx=fmaxf(mx,lg[w]);
  float e[8]; float sum=0;
  #pragma unroll
  for(int w=0;w<8;w++){ e[w]=__expf(lg[w]-mx); sum+=e[w]; }
  #pragma unroll
  for(int w=0;w<8;w++) e[w]=rbf(e[w]/sum);
  float a[DH];
  #pragma unroll
  for(int d=0;d<DH;d++) a[d]=0;
  #pragma unroll
  for(int w=0;w<8;w++){ int p=s-2*w; if(p<0)p=0; const u16* v=qkv+(size_t)p*1920+1280+h*DH;
    #pragma unroll
    for(int d=0;d<DH;d+=2){ unsigned t=*(const unsigned*)(v+d); a[d]+=e[w]*lo16(t); a[d+1]+=e[w]*hi16(t); } }
  #pragma unroll
  for(int d=0;d<DH;d+=2) *(unsigned*)(o+(size_t)s*D_+h*DH+d)=pk2(a[d],a[d+1]);
}

#define K3_ASZ 16384
#define K3_BSZ 20480
#define K3_STG (K3_ASZ+K3_BSZ)
#define K3_NS 4
__global__ __launch_bounds__(512,2) void k3(const u16* __restrict__ x, const u16* __restrict__ o, const u16* __restrict__ h,
    const u16* __restrict__ wo, const u16* __restrict__ wd, u16* __restrict__ out){
  __shared__ __attribute__((aligned(16))) char smem[K3_NS*K3_STG];
  const int tid=threadIdx.x, lane=tid&63, wid=tid>>6;
  const int nt = blockIdx.x >> 5, mt = (blockIdx.x&7)*4 + ((blockIdx.x>>3)&3);
  const int m0=mt*64, n0=nt*80;
  int aoff[2];
  #pragma unroll
  for(int i=0;i<2;i++){ int id=wid*2+i; int hf=id>>3; int r=(id&7)*8+(lane>>3); aoff[i]=(r<<16) | (hf*64 + ((lane&7)^((r>>1)&7))*8); }
  int boff[3];
  #pragma unroll
  for(int i=0;i<3;i++){ int idx = wid + 8*i; int L=(idx*64+lane)*16; boff[i]=(L/160)*D_ + n0 + (L%160)/2; }
  auto issue=[&](int kt, char* buf){
    const u16* A; int lda, k0; const u16* B;
    if(kt<5){ A=o; lda=D_; k0=kt*128; B=wo+(size_t)k0*D_; } else { A=h; lda=F_; k0=(kt-5)*128; B=wd+(size_t)k0*D_; }
    const bool last = (kt==18);
    #pragma unroll
    for(int i=0;i<2;i++){
      int koff = aoff[i]&0xffff; if(last && koff>=64) koff-=64;
      glds16((const void*)(A+(size_t)(m0+(aoff[i]>>16))*lda+k0+koff), LADDR(buf+(wid*2+i)*1024));
    }
    #pragma unroll
    for(int i=0;i<3;i++){ if(wid+8*i<20){
      int bo=boff[i]; if(last && (wid+8*i)>=10) bo-=64*D_;
      glds16((const void*)(B+bo), LADDR(buf+K3_ASZ+(wid+8*i)*1024)); } }
  };
  f32x4 acc[2][5];
  #pragma unroll
  for(int a=0;a<2;a++)
    #pragma unroll
    for(int b=0;b<5;b++) acc[a][b]=(f32x4){0,0,0,0};
  const int wr=wid&1, ks=wid>>1;
  const int g=lane>>4, l15=lane&15, q=(lane&15)>>2, p=lane&3;
  issue(0,smem); issue(1,smem+K3_STG); issue(2,smem+2*K3_STG);
  #pragma unroll
  for(int kt=0;kt<19;++kt){
    char* cur=smem+(kt%K3_NS)*K3_STG;
    if(kt<17){ if(wid<4) asm volatile("s_waitcnt vmcnt(10)":::"memory"); else asm volatile("s_waitcnt vmcnt(8)":::"memory"); }
    else if(kt==17){ if(wid<4) asm volatile("s_waitcnt vmcnt(5)":::"memory"); else asm volatile("s_waitcnt vmcnt(4)":::"memory"); }
    else asm volatile("s_waitcnt vmcnt(0)":::"memory");
    __builtin_amdgcn_s_barrier();
    if(kt+3<19) issue(kt+3, smem+((kt+3)%K3_NS)*K3_STG);
    if(kt<18 || ks<2){
      s16x8 af[2];
      #pragma unroll
      for(int a=0;a<2;a++){ int row=wr*32+a*16+l15; int ch=(ks&1)*4+g; af[a]=*(const s16x8*)(cur+(ks>>1)*8192+row*128+((ch^((row>>1)&7))*16)); }
      #pragma unroll
      for(int b=0;b<5;b++){
        int row=ks*32+8*g+q;
        s16x4 t0=tr_read(cur+K3_ASZ+row*160+b*32+p*8);
        s16x4 t1=tr_read(cur+K3_ASZ+(row+4)*160+b*32+p*8);
        s16x8 bf=__builtin_shufflevector(t0,t1,0,1,2,3,4,5,6,7);
        #pragma unroll
        for(int a=0;a<2;a++) acc[a][b]=__builtin_amdgcn_mfma_f32_16x16x32_bf16(bf,af[a],acc[a][b],0,0,0);
      }
    }
    asm volatile("s_waitcnt lgkmcnt(0)":::"memory");
  }
  __syncthreads();
  float* red=(float*)smem;
  if(ks>0){
    #pragma unroll
    for(int a=0;a<2;a++)
      #pragma unroll
      for(int b=0;b<5;b++) *(f32x4*)(red + ((((ks-1)*2+wr)*10+a*5+b)*64+lane)*4)=acc[a][b];
  }
  __syncthreads();
  if(ks==0){
    #pragma unroll
    for(int a=0;a<2;a++){
      int m=m0+wr*32+a*16+l15;
      #pragma unroll
      for(int b=0;b<5;b++){
        f32x4 r=acc[a][b];
        #pragma unroll
        for(int z=0;z<3;z++) r+=*(const f32x4*)(red + (((z*2+wr)*10+a*5+b)*64+lane)*4);
        int n=n0+b*16+g*4;
        u32x2 xv=*(const u32x2*)(x+(size_t)m*D_+n);
        u32x2 ov; ov[0]=pk2(lo16(xv[0])+r[0], hi16(xv[0])+r[1]);
        ov[1]=pk2(lo16(xv[1])+r[2], hi16(xv[1])+r[3]);
        *(u32x2*)(out+(size_t)m*D_+n)=ov;
      }
    }
  }
}

extern "C" void kernel_launch(void* const* d_in, const int* in_sizes, int n_in,
                              void* d_out, int out_size, void* d_ws, size_t ws_size,
                              hipStream_t stream) {
  const u16* x=(const u16*)d_in[0]; const u16* wqkv=(const u16*)d_in[1]; const u16* wo=(const u16*)d_in[2];
  const u16* wg=(const u16*)d_in[4]; const u16* wu=(const u16*)d_in[5]; const u16* wd=(const u16*)d_in[6];
  u16* ws=(u16*)d_ws;
  u16* qkv=ws; u16* h=qkv+S_*1920; u16* o=h+S_*F_; u16* xn=o+S_*D_;
  ln_kernel<<<128,256,0,stream>>>(x,xn);
  k1<<<8*28,512,0,stream>>>(xn,wqkv,wg,wu,qkv,h);
  attn_kernel<<<S_/8,256,0,stream>>>(qkv,o);
  k3<<<256,512,0,stream>>>(x,o,h,wo,wd,(u16*)d_out);
}
```

```cpp
#include <hip/hip_runtime.h>
#include <hip/hip_bf16.h>
#include <stdint.h>

typedef unsigned short u16;
typedef short s16x8 __attribute__((ext_vector_type(8)));
typedef short s16x4 __attribute__((ext_vector_type(4)));
typedef float f32x4 __attribute__((ext_vector_type(4)));
typedef unsigned u32x4 __attribute__((ext_vector_type(4)));
typedef unsigned u32x2 __attribute__((ext_vector_type(2)));
#define LDS_PTR(T) __attribute__((address_space(3))) T*

#define S_ 2048
#define D_ 640
#define F_ 1728
#define DH 20

__device__ __forceinline__ float bf2f(u16 v){ return __uint_as_float(((unsigned)v)<<16); }
__device__ __forceinline__ u16 f2bf(float f){ __hip_bfloat16 b = __float2bfloat16(f); return *(u16*)&b; }
__device__ __forceinline__ float rbf(float f){ return bf2f(f2bf(f)); }
__device__ __forceinline__ unsigned pk2(float a, float b){ return (unsigned)f2bf(a) | ((unsigned)f2bf(b)<<16); }
__device__ __forceinline__ float lo16(unsigned v){ return __uint_as_float(v<<16); }
__device__ __forceinline__ float hi16(unsigned v){ return __uint_as_float(v&0xffff0000u); }

__device__ __forceinline__ s16x4 tr_read(const char* p){
  return __builtin_amdgcn_ds_read_tr16_b64_v4i16((LDS_PTR(s16x4))p);
}

__device__ __forceinline__ void glds16(const void* gsrc, unsigned lds_dst){
  unsigned keep;
  asm volatile("s_mov_b32 %0, m0\n\ts_mov_b32 m0, %2\n\ts_nop 0\n\tglobal_load_lds_dwordx4 %1, off\n\ts_mov_b32 m0, %0" : "=&s"(keep) : "v"(gsrc), "s"(lds_dst) : "memory");
}
#define LADDR(p) ((unsigned)__builtin_amdgcn_readfirstlane((int)(unsigned)(size_t)(p)))
__global__ __launch_bounds__(256) void ln_kernel(const u16* __restrict__ x, u16* __restrict__ xn){
  int lane=threadIdx.x&63; int row=blockIdx.x*16+(threadIdx.x>>6)*4+(lane>>4); int l16=lane&15;
  const u16* xr=x+(size_t)row*D_;
  float v[40]; float s=0;
  #pragma unroll
  for(int i=0;i<5;i++){ u32x4 d=*(const u32x4*)(xr+(l16+16*i)*8);
    #pragma unroll
    for(int j=0;j<4;j++){ v[i*8+2*j]=lo16(d[j]); v[i*8+2*j+1]=hi16(d[j]); } }
  #pragma unroll
  for(int i=0;i<40;i++) s+=v[i];
  #pragma unroll
  for(int o=8;o>0;o>>=1) s+=__shfl_xor(s,o);
  float mean=s*(1.0f/D_); float q=0;
  #pragma unroll
  for(int i=0;i<40;i++){ float d=v[i]-mean; q+=d*d; }
  #pragma unroll
  for(int o=8;o>0;o>>=1) q+=__shfl_xor(q,o);
  float mb=rbf(mean), vb=rbf(q*(1.0f/D_));
  float ri=1.0f/rbf(sqrtf(rbf(vb+1e-5f)));
  #pragma unroll
  for(int i=0;i<5;i++){ u32x4 o;
    #pragma unroll
    for(int j=0;j<4;j++) o[j]=pk2(rbf(v[i*8+2*j]-mb)*ri, rbf(v[i*8+2*j+1]-mb)*ri);
    *(u32x4*)(xn+(size_t)row*D_+(l16+16*i)*8)=o; }
}
#define K1_ASZ 32768
#define K1_BSZ 24576
#define K1_STG (K1_ASZ+K1_BSZ)
__global__ __launch_bounds__(512,2) void k1(const u16* __restrict__ xn, const u16* __restrict__ wqkv,
    const u16* __restrict__ wg, const u16* __restrict__ wu, u16* __restrict__ qkv, u16* __restrict__ h){
  __shared__ __attribute__((aligned(16))) char smem[2*K1_STG];
  const int tid=threadIdx.x, lane=tid&63, wid=tid>>6;
  const int bid=blockIdx.x;
  const int xcd=bid&7, idx=bid>>3;
  const int mt=(xcd&1)*4+(idx&3), nt=(xcd>>1)*7+(idx>>2);
  const int m0 = mt*256;
  const bool isq = nt<10;
  const u16* bsrc[3]; int ldb = isq?1920:F_;
  #pragma unroll
  for(int i=0;i<3;i++){
    int L = ((wid*3+i)*64+lane)*16;
    int row = L/384, pc=(L%384)/16;
    int sw = ((row>>1)&1) | (((row>>3)&1)<<1);
    int c = (pc>>1)^sw;
    int col = c*16 + (pc&1)*8;
    const u16* p;
    if(isq) p = wqkv + nt*192 + col;
    else { int j=nt-10; int wc=col/96, cc=col%96; p = (cc<48)? (wg + j*96+wc*48+cc) : (wu + j*96+wc*48+cc-48); }
    bsrc[i] = p + (size_t)row*ldb;
  }
  const u16* asrc[4];
  #pragma unroll
  for(int i=0;i<4;i++){ int r=(wid*4+i)*8+(lane>>3); asrc[i]=xn+(size_t)(m0+r)*D_+(((lane&7)^((r>>1)&7))*8); }
  auto issue=[&](int kt, char* buf){
    #pragma unroll
    for(int i=0;i<3;i++)
      glds16((const void*)(bsrc[i] + (size_t)kt*64*ldb), LADDR(buf + K1_ASZ + (wid*3+i)*1024));
    #pragma unroll
    for(int i=0;i<4;i++)
      glds16((const void*)(asrc[i]+kt*64), LADDR(buf+(wid*4+i)*1024));
  };
  f32x4 acc[4][6];
  #pragma unroll
  for(int a=0;a<4;a++)
    #pragma unroll
    for(int b=0;b<6;b++) acc[a][b]=(f32x4){0,0,0,0};
  const int wr=wid>>1, wc=wid&1;
  const int g=lane>>4, l15=lane&15, q=(lane&15)>>2, p=lane&3;
  issue(0,smem);
  #pragma unroll
  for(int kt=0; kt<10; ++kt){
    char* cur = smem + (kt&1)*K1_STG;
    asm volatile("s_waitcnt vmcnt(0)":::"memory");
    __builtin_amdgcn_s_barrier();
    if(kt+1<10) issue(kt+1, smem+((kt+1)&1)*K1_STG);
    #pragma unroll
    for(int ks=0; ks<2; ++ks){
      s16x8 af[4];
      #pragma unroll
      for(int a=0;a<4;a++){
        int row = wr*64 + a*16 + l15;
        int ch = ks*4+g;
        af[a] = *(const s16x8*)(cur + row*128 + ((ch^((row>>1)&7))*16));
      }
      #pragma unroll
      for(int b=0;b<6;b++){
        int c = wc*6+b;
        s16x4 t0,t1;
        { int row=ks*32+8*g+q; int sw=((row>>1)&1)|(((row>>3)&1)<<1);
          t0=tr_read(cur+K1_ASZ+row*384+((c^sw)*32)+p*8); }
        { int row=ks*32+8*g+4+q; int sw=((row>>1)&1)|(((row>>3)&1)<<1);
          t1=tr_read(cur+K1_ASZ+row*384+((c^sw)*32)+p*8); }
        s16x8 bf = __builtin_shufflevector(t0,t1,0,1,2,3,4,5,6,7);
        #pragma unroll
        for(int a=0;a<4;a++)
          acc[a][b]=__builtin_amdgcn_mfma_f32_16x16x32_bf16(bf, af[a], acc[a][b],0,0,0);
      }
    }
    asm volatile("s_waitcnt lgkmcnt(0)":::"memory");
  }
  __builtin_amdgcn_s_barrier();
  char* eb = smem + wid*(64*208);
  if(isq){
    #pragma unroll
    for(int a=0;a<4;a++)
      #pragma unroll
      for(int b=0;b<6;b++){
        u32x2 o; o[0]=pk2(acc[a][b][0],acc[a][b][1]); o[1]=pk2(acc[a][b][2],acc[a][b][3]);
        *(u32x2*)(eb + (a*16+l15)*208 + b*32 + g*8) = o;
      }
    __builtin_amdgcn_s_waitcnt(0); __builtin_amdgcn_wave_barrier();
    #pragma unroll
    for(int i=0;i<12;i++){ int id=i*64+lane; int r=id/12, c=id%12;
      u32x4 v=*(const u32x4*)(eb + r*208 + c*16);
      *(u32x4*)(qkv + (size_t)(m0+wr*64+r)*1920 + nt*192 + wc*96 + c*8)=v; }
  } else {
    int j=nt-10;
    #pragma unroll
    for(int a=0;a<4;a++)
      #pragma unroll
      for(int b=0;b<3;b++){
        float r[4];
        #pragma unroll
        for(int e=0;e<4;e++){ float gv=rbf(acc[a][b][e]); gv=gv>0.f?gv:0.f; r[e]=gv*rbf(acc[a][b+3][e]); }
        u32x2 o; o[0]=pk2(r[0],r[1]); o[1]=pk2(r[2],r[3]);
        *(u32x2*)(eb + (a*16+l15)*208 + b*32 + g*8) = o;
      }
    __builtin_amdgcn_s_waitcnt(0); __builtin_amdgcn_wave_barrier();
    #pragma unroll
    for(int i=0;i<6;i++){ int id=i*64+lane; int r=id/6, c=id%6;
      u32x4 v=*(const u32x4*)(eb + r*208 + c*16);
      *(u32x4*)(h + (size_t)(m0+wr*64+r)*F_ + j*96 + wc*48 + c*8)=v; }
  }
}

__global__ __launch_bounds__(256) void attn_kernel(const u16* __restrict__ qkv, u16* __restrict__ o){
  int s = blockIdx.x*8 + (threadIdx.x>>5); int h = threadIdx.x&31;
  const u16* qp = qkv + (size_t)s*1920 + h*DH;
  float qv[DH];
  #pragma unroll
  for(int d=0;d<DH;d+=2){ unsigned t=*(const unsigned*)(qp+d); qv[d]=lo16(t); qv[d+1]=hi16(t); }
  float lg[8]; const float scale = 0.2236328125f;
  #pragma unroll
  for(int w=0;w<8;w++){
    int p = s-2*w;
    if(p>=0){
      const u16* k = qkv + (size_t)p*1920 + 640 + h*DH;
      float a=0;
      #pragma unroll
      for(int d=0;d<DH;d+=2){ unsigned t=*(const unsigned*)(k+d); a+=qv[d]*lo16(t); a+=qv[d+1]*hi16(t); }
      lg[w]=rbf(rbf(a)*scale);
    } else lg[w]=-1e9f;
  }
  float mx=lg[0];
  #pragma unroll
  for(int w=1;w<8;w++) mx=fmaxf(mx,lg[w]);
  float e[8]; float sum=0;
  #pragma unroll
  for(int w=0;w<8;w++){ e[w]=__expf(lg[w]-mx); sum+=e[w]; }
  #pragma unroll
  for(int w=0;w<8;w++) e[w]=rbf(e[w]/sum);
  float a[DH];
  #pragma unroll
  for(int d=0;d<DH;d++) a[d]=0;
  #pragma unroll
  for(int w=0;w<8;w++){ int p=s-2*w; if(p<0)p=0; const u16* v=qkv+(size_t)p*1920+1280+h*DH;
    #pragma unroll
    for(int d=0;d<DH;d+=2){ unsigned t=*(const unsigned*)(v+d); a[d]+=e[w]*lo16(t); a[d+1]+=e[w]*hi16(t); } }
  #pragma unroll
  for(int d=0;d<DH;d+=2) *(unsigned*)(o+(size_t)s*D_+h*DH+d)=pk2(a[d],a[d+1]);
}

#define K3_ASZ 16384
#define K3_BSZ 20480
#define K3_STG (K3_ASZ+K3_BSZ)
#define K3_NS 4
__global__ __launch_bounds__(512,2) void k3(const u16* __restrict__ x, const u16* __restrict__ o, const u16* __restrict__ h,
    const u16* __restrict__ wo, const u16* __restrict__ wd, u16* __restrict__ out){
  __shared__ __attribute__((aligned(16))) char smem[K3_NS*K3_STG];
  const int tid=threadIdx.x, lane=tid&63, wid=tid>>6;
  const int nt = blockIdx.x >> 5, mt = (blockIdx.x&7)*4 + ((blockIdx.x>>3)&3);
  const int m0=mt*64, n0=nt*80;
  int aoff[2];
  #pragma unroll
  for(int i=0;i<2;i++){ int id=wid*2+i; int hf=id>>3; int r=(id&7)*8+(lane>>3); aoff[i]=(r<<16) | (hf*64 + ((lane&7)^((r>>1)&7))*8); }
  int boff[3];
  #pragma unroll
  for(int i=0;i<3;i++){ int idx = wid + 8*i; int L=(idx*64+lane)*16; boff[i]=(L/160)*D_ + n0 + (L%160)/2; }
  auto issue=[&](int kt, char* buf){
    const u16* A; int lda, k0; const u16* B;
    if(kt<5){ A=o; lda=D_; k0=kt*128; B=wo+(size_t)k0*D_; } else { A=h; lda=F_; k0=(kt-5)*128; B=wd+(size_t)k0*D_; }
    const bool last = (kt==18);
    #pragma unroll
    for(int i=0;i<2;i++){
      int koff = aoff[i]&0xffff; if(last && koff>=64) koff-=64;
      glds16((const void*)(A+(size_t)(m0+(aoff[i]>>16))*lda+k0+koff), LADDR(buf+(wid*2+i)*1024));
    }
    #pragma unroll
    for(int i=0;i<3;i++){ if(wid+8*i<20){
      int bo=boff[i]; if(last && (wid+8*i)>=10) bo-=64*D_;
      glds16((const void*)(B+bo), LADDR(buf+K3_ASZ+(wid+8*i)*1024)); } }
  };
  f32x4 acc[2][5];
  #pragma unroll
  for(int a=0;a<2;a++)
    #pragma unroll
    for(int b=0;b<5;b++) acc[a][b]=(f32x4){0,0,0,0};
  const int wr=wid&1, ks=wid>>1;
  const int g=lane>>4, l15=lane&15, q=(lane&15)>>2, p=lane&3;
  issue(0,smem); issue(1,smem+K3_STG); issue(2,smem+2*K3_STG);
  #pragma unroll
  for(int kt=0;kt<19;++kt){
    char* cur=smem+(kt%K3_NS)*K3_STG;
    if(kt<17){ if(wid<4) asm volatile("s_waitcnt vmcnt(10)":::"memory"); else asm volatile("s_waitcnt vmcnt(8)":::"memory"); }
    else if(kt==17){ if(wid<4) asm volatile("s_waitcnt vmcnt(5)":::"memory"); else asm volatile("s_waitcnt vmcnt(4)":::"memory"); }
    else asm volatile("s_waitcnt vmcnt(0)":::"memory");
    __builtin_amdgcn_s_barrier();
    if(kt+3<19) issue(kt+3, smem+((kt+3)%K3_NS)*K3_STG);
    if(kt<18 || ks<2){
      s16x8 af[2];
      #pragma unroll
      for(int a=0;a<2;a++){ int row=wr*32+a*16+l15; int ch=(ks&1)*4+g; af[a]=*(const s16x8*)(cur+(ks>>1)*8192+row*128+((ch^((row>>1)&7))*16)); }
      #pragma unroll
      for(int b=0;b<5;b++){
        int row=ks*32+8*g+q;
        s16x4 t0=tr_read(cur+K3_ASZ+row*160+b*32+p*8);
        s16x4 t1=tr_read(cur+K3_ASZ+(row+4)*160+b*32+p*8);
        s16x8 bf=__builtin_shufflevector(t0,t1,0,1,2,3,4,5,6,7);
        #pragma unroll
        for(int a=0;a<2;a++) acc[a][b]=__builtin_amdgcn_mfma_f32_16x16x32_bf16(bf,af[a],acc[a][b],0,0,0);
      }
    }
    asm volatile("s_waitcnt lgkmcnt(0)":::"memory");
  }
  __syncthreads();
  float* red=(float*)smem;
  if(ks>0){
    #pragma unroll
    for(int a=0;a<2;a++)
      #pragma unroll
      for(int b=0;b<5;b++) *(f32x4*)(red + ((((ks-1)*2+wr)*10+a*5+b)*64+lane)*4)=acc[a][b];
  }
  __syncthreads();
  if(ks==0){
    #pragma unroll
    for(int a=0;a<2;a++){
      int m=m0+wr*32+a*16+l15;
      #pragma unroll
      for(int b=0;b<5;b++){
        f32x4 r=acc[a][b];
        #pragma unroll
        for(int z=0;z<3;z++) r+=*(const f32x4*)(red + (((z*2+wr)*10+a*5+b)*64+lane)*4);
        int n=n0+b*16+g*4;
        u32x2 xv=*(const u32x2*)(x+(size_t)m*D_+n);
        u32x2 ov; ov[0]=pk2(lo16(xv[0])+r[0], hi16(xv[0])+r[1]);
        ov[1]=pk2(lo16(xv[1])+r[2], hi16(xv[1])+r[3]);
        *(u32x2*)(out+(size_t)m*D_+n)=ov;
      }
    }
  }
}

extern "C" void kernel_launch(void* const* d_in, const int* in_sizes, int n_in,
                              void* d_out, int out_size, void* d_ws, size_t ws_size,
                              hipStream_t stream) {
  const u16* x=(const u16*)d_in[0]; const u16* wqkv=(const u16*)d_in[1]; const u16* wo=(const u16*)d_in[2];
  const u16* wg=(const u16*)d_in[4]; const u16* wu=(const u16*)d_in[5]; const u16* wd=(const u16*)d_in[6];
  u16* ws=(u16*)d_ws;
  u16* qkv=ws; u16* h=qkv+S_*1920; u16* o=h+S_*F_; u16* xn=o+S_*D_;
  ln_kernel<<<128,256,0,stream>>>(x,xn);
  k1<<<8*28,512,0,stream>>>(xn,wqkv,wg,wu,qkv,h);
  attn_kernel<<<S_/8,256,0,stream>>>(qkv,o);
  k3<<<256,512,0,stream>>>(x,o,h,wo,wd,(u16*)d_out);
}
```
